```python
import torch

def _softmax(x, axis=-1):
    return torch.softmax(x, dim=axis)
def _gelu(x):
    return torch.nn.functional.gelu(x, approximate="tanh")
def _silu(x):
    return torch.nn.functional.silu(x)
def _sigmoid(x):
    return torch.sigmoid(x)
def _layernorm(x, axis=-1, eps=1e-5):
    m = x.mean(dim=axis, keepdim=True)
    v = x.var(dim=axis, keepdim=True, unbiased=False)
    return (x - m) / torch.sqrt(v + eps)
def _scatter_add(tgt, idx, src, axis=0):
    return tgt.clone().index_add_(axis, idx, src)
def _segment_sum(src, seg, n):
    out = torch.zeros((n,) + src.shape[1:], dtype=src.dtype)
    return out.index_add_(0, seg, src)
def _pad(x, pw):
    flat = tuple(v for pair in reversed(pw) for v in pair)
    return torch.nn.functional.pad(x, flat)

kernel_name = 'gated_mix__mha_block_cached__gqa_block_cached__decode'

def setup_inputs():

    _w = (lambda a: (lambda t: t.to(torch.bfloat16) if t.is_floating_point() else t)(torch.as_tensor(a)))
    def _A_setup():
        B, Sq, D = 1, 1, 448
        H, Sk, Dh = 16, 8192, 256
        return {
            "x": _w((lambda *s: torch.randn(*s, dtype=torch.bfloat16) * 0.05)(B, Sq, D)),
            "k_cache": _w((lambda *s: torch.randn(*s, dtype=torch.bfloat16) * 3.2)(B, H, Sk, Dh)),
            "v_cache": _w((lambda *s: torch.randn(*s, dtype=torch.bfloat16) * 0.1)(B, H, Sk, Dh)),
            "wq": _w((lambda *s: torch.randn(*s, dtype=torch.bfloat16) * 3.2)(D, H * Dh) * (D ** -0.5)),
            "wo": _w((lambda *s: torch.randn(*s, dtype=torch.bfloat16) * 0.1)(H * Dh, D) * ((H * Dh) ** -0.5)),
        }
    _A_in = _A_setup()

    def _B_setup():
        B, Sq, D = 1, 1, 448
        Hq, Hkv, Sk, Dh = 16, 8, 8192, 256
        assert Hq % Hkv == 0, (Hq, Hkv)
        return {
            "x": _w((lambda *s: torch.randn(*s, dtype=torch.bfloat16) * 0.05)(B, Sq, D)),
            "k_cache": _w((lambda *s: torch.randn(*s, dtype=torch.bfloat16) * 3.2)(B, Hkv, Sk, Dh)),
            "v_cache": _w((lambda *s: torch.randn(*s, dtype=torch.bfloat16) * 0.05)(B, Hkv, Sk, Dh)),
            "wq": _w((lambda *s: torch.randn(*s, dtype=torch.bfloat16) * 3.2)(D, Hq * Dh) * (D ** -0.5)),
            "wo": _w((lambda *s: torch.randn(*s, dtype=torch.bfloat16) * 0.05)(Hq * Dh, D) * ((Hq * Dh) ** -0.5)),
        }
    _B_in = _B_setup()

    return {
        "A_x": _A_in["x"],
        "A_k_cache": _A_in["k_cache"],
        "A_v_cache": _A_in["v_cache"],
        "A_wq": _A_in["wq"],
        "A_wo": _A_in["wo"],
        "B_x": _B_in["x"],
        "B_k_cache": _B_in["k_cache"],
        "B_v_cache": _B_in["v_cache"],
        "B_wq": _B_in["wq"],
        "B_wo": _B_in["wo"],
        "gate_w": _w((lambda *s: torch.randn(*s, dtype=torch.bfloat16) * 0.1)(448) * 16.0),
    }

def reference(A_x, A_k_cache, A_v_cache, A_wq, A_wo, B_x, B_k_cache, B_v_cache, B_wq, B_wo, gate_w):
    def _A_ref(x, k_cache, v_cache, wq, wo):
        B, Sq, D = x.shape
        H, Dh = 16, 256
        q = (lambda x, *d: x.permute(*d))((x @ wq).reshape(B, Sq, H, Dh), 0, 2, 1, 3)
        scores = torch.einsum("bhqd,bhkd->bhqk", q, k_cache) * (Dh ** -0.5)
        p = _softmax(scores, axis=-1)
        o = torch.einsum("bhqk,bhkd->bhqd", p, v_cache)
        return (lambda x, *d: x.permute(*d))(o, 0, 2, 1, 3).reshape(B, Sq, H * Dh) @ wo

    def _B_ref(x, k_cache, v_cache, wq, wo):
        B, Sq, D = x.shape
        Hq, Dh = 16, 256
        Hkv = k_cache.shape[1]
        rep = Hq // Hkv
        q = (lambda x, *d: x.permute(*d))((x @ wq).reshape(B, Sq, Hq, Dh), 0, 2, 1, 3)
        k = (lambda x, n, axis: torch.repeat_interleave(x, n, dim=axis))(k_cache, rep, axis=1)
        v = (lambda x, n, axis: torch.repeat_interleave(x, n, dim=axis))(v_cache, rep, axis=1)
        scores = torch.einsum("bhqd,bhkd->bhqk", q, k) * (Dh ** -0.5)
        p = _softmax(scores, axis=-1)
        o = torch.einsum("bhqk,bhkd->bhqd", p, v)
        return (lambda x, *d: x.permute(*d))(o, 0, 2, 1, 3).reshape(B, Sq, Hq * Dh) @ wo

    x = A_x
    g = _sigmoid((x * gate_w).sum(-1, keepdims=True))
    a = _A_ref(x, A_k_cache, A_v_cache, A_wq, A_wo)
    b = _B_ref(x, B_k_cache, B_v_cache, B_wq, B_wo)
    return g * a + (1 - g) * b
```

```cpp
#include <hip/hip_runtime.h>
#include <hip/hip_bf16.h>
#include <stdint.h>

#define GAS __attribute__((address_space(1)))
#define LAS __attribute__((address_space(3)))
typedef unsigned v4u __attribute__((ext_vector_type(4)));
typedef float f32x4 __attribute__((ext_vector_type(4)));
typedef float f32x2 __attribute__((ext_vector_type(2)));
typedef __bf16 bf16x2 __attribute__((ext_vector_type(2)));

#define KV_NT 1
#define WIN 32

constexpr int D = 448, HD = 4096, SK = 8192, DH = 256;

__device__ __forceinline__ float dot2(unsigned k, unsigned q, float acc) {
  return __builtin_amdgcn_fdot2_f32_bf16(__builtin_bit_cast(bf16x2, k), __builtin_bit_cast(bf16x2, q), acc, false);
}
__device__ __forceinline__ float bflo(unsigned u) { return __builtin_bit_cast(float, u << 16); }
__device__ __forceinline__ float bfhi(unsigned u) { return __builtin_bit_cast(float, u & 0xffff0000u); }
__device__ __forceinline__ float bf2f(unsigned short b) { return __builtin_bit_cast(float, (unsigned)b << 16); }
__device__ __forceinline__ unsigned short f2bf(float f) { return __builtin_bit_cast(unsigned short, (__bf16)f); }
__device__ __forceinline__ float rbf(float f) { return (float)(__bf16)f; }
template <int CTRL> __device__ __forceinline__ float dpp(float x) {
  return __builtin_bit_cast(float, __builtin_amdgcn_mov_dpp(__builtin_bit_cast(int, x), CTRL, 0xf, 0xf, true));
}
constexpr int XOR1 = 0xB1, XOR2 = 0x4E, XOR7 = 0x141, XOR8 = 0x128;
typedef __amdgpu_buffer_rsrc_t rsrc_t;
__device__ __forceinline__ v4u ldkv(rsrc_t r, int voff, int soff) {
  return __builtin_amdgcn_raw_buffer_load_b128(r, voff, soff, KV_NT ? 2 : 0);
}
__device__ __forceinline__ rsrc_t mkrsrc(const void* p, unsigned bytes) {
  return __builtin_amdgcn_make_buffer_rsrc((void*)p, 0, bytes, 0x00020000);
}
__device__ __forceinline__ float swap16_add(float a, float b) {
  auto s = __builtin_amdgcn_permlane16_swap(__float_as_uint(a), __float_as_uint(b), false, false);
  return __uint_as_float(s[0]) + __uint_as_float(s[1]);
}
__device__ __forceinline__ float swap32_add(float a, float b) {
  auto s = __builtin_amdgcn_permlane32_swap(__float_as_uint(a), __float_as_uint(b), false, false);
  return __uint_as_float(s[0]) + __uint_as_float(s[1]);
}
template <int CTRL> __device__ __forceinline__ float tr_add(float a, float b, bool bit) {
  const float keep = bit ? b : a, send = bit ? a : b;
  return keep + dpp<CTRL>(send);
}
__device__ __forceinline__ float wave_max(float x) {
  x = fmaxf(x, dpp<XOR1>(x)); x = fmaxf(x, dpp<XOR2>(x)); x = fmaxf(x, dpp<XOR7>(x)); x = fmaxf(x, dpp<XOR8>(x));
  auto s = __builtin_amdgcn_permlane16_swap(__float_as_uint(x), __float_as_uint(x), false, false);
  x = fmaxf(__uint_as_float(s[0]), __uint_as_float(s[1]));
  auto t = __builtin_amdgcn_permlane32_swap(__float_as_uint(x), __float_as_uint(x), false, false);
  return fmaxf(__uint_as_float(t[0]), __uint_as_float(t[1]));
}
__device__ __forceinline__ float wave_sum(float x) {
  x += dpp<XOR1>(x); x += dpp<XOR2>(x); x += dpp<XOR7>(x); x += dpp<XOR8>(x);
  x = swap16_add(x, x);
  return swap32_add(x, x);
}

constexpr size_t WS_Q = 0;
constexpr size_t WS_PART = 65536;
constexpr size_t WS_ML = WS_PART + 8 * 32 * 32 * 32 * 4;
constexpr size_t WS_Y = WS_ML + 8 * 4 * 32 * 2 * 4;

__global__ void __launch_bounds__(256) k_qproj(const unsigned short* x, const unsigned short* wqA, const unsigned short* wqB, unsigned char* ws) {
  __shared__ float red[4][32];
  __shared__ float xs[D];
  const int b = blockIdx.x, t = b & 7, j = b >> 3, tid = threadIdx.x, lane = tid & 63, w = tid >> 6;
  const unsigned short* wq = (j < 16) ? wqA : wqB;
  const int col0 = 512 * t + 32 * (j & 15);
  for (int i = tid; i < D; i += 256) xs[i] = bf2f(x[i]);
  v4u wv[7];
  const int r0 = w * 112 + (lane >> 2);
#pragma unroll
  for (int i = 0; i < 7; ++i) wv[i] = *(const GAS v4u*)(wq + (size_t)(r0 + 16 * i) * HD + col0 + 8 * (lane & 3));
  __syncthreads();
  float acc[8];
#pragma unroll
  for (int k = 0; k < 8; ++k) acc[k] = 0.f;
#pragma unroll
  for (int i = 0; i < 7; ++i) {
    const float xv = xs[r0 + 16 * i];
#pragma unroll
    for (int k = 0; k < 4; ++k) { acc[2 * k] = fmaf(xv, bflo(wv[i][k]), acc[2 * k]); acc[2 * k + 1] = fmaf(xv, bfhi(wv[i][k]), acc[2 * k + 1]); }
  }
#pragma unroll
  for (int k = 0; k < 8; ++k) {
    float v = acc[k];
    v += __shfl_xor(v, 4); v += __shfl_xor(v, 8); v += __shfl_xor(v, 16); v += __shfl_xor(v, 32);
    if (lane < 4) red[w][8 * lane + k] = v;
  }
  __syncthreads();
  if (tid < 32) {
    const float q = (red[0][tid] + red[1][tid]) + (red[2][tid] + red[3][tid]);
    ((unsigned short*)(ws + WS_Q))[t * 1024 + j * 32 + tid] = f2bf(q);
  }
}

template <int NH>
__device__ __forceinline__ void stream_wave(rsrc_t kp, rsrc_t vp, int voff, const v4u (&q)[NH], LAS float* pbuf, LAS float* obuf, LAS float* ml, int lane) {
  constexpr int W = WIN;
  constexpr int STR = 1024;
  v4u a[W];
#pragma unroll
  for (int n = 0; n < W; ++n) { a[n] = n < 32 ? ldkv(kp, voff, n * STR) : ldkv(vp, voff, (n - 32) * STR);
    __builtin_amdgcn_sched_barrier(0); }
  float lv[NH][6];
  const bool b3 = lane & 8, b2 = lane & 4, b1 = lane & 2, b0 = lane & 1;
#pragma unroll
  for (int n = 0; n < 32; ++n) {
    const v4u cur = a[n % W];
    float x[NH];
#pragma unroll
    for (int h = 0; h < NH; ++h) {
      float d = dot2(cur[0], q[h][0], 0.f);
      d = dot2(cur[1], q[h][1], d); d = dot2(cur[2], q[h][2], d); d = dot2(cur[3], q[h][3], d);
      x[h] = d;
    }
    __builtin_amdgcn_sched_barrier(0);
    if (n + W < 64) a[n % W] = n + W < 32 ? ldkv(kp, voff, (n + W) * STR) : ldkv(vp, voff, (n + W - 32) * STR);
#pragma unroll
    for (int h = 0; h < NH; ++h) {
      float v = x[h];
      if (!(n & 1)) { lv[h][0] = v; continue; }
      v = swap16_add(lv[h][0], v);
      if (!(n & 2)) { lv[h][1] = v; continue; }
      v = tr_add<XOR8>(lv[h][1], v, b3);
      if (!(n & 4)) { lv[h][2] = v; continue; }
      v = tr_add<XOR7>(lv[h][2], v, b2);
      if (!(n & 8)) { lv[h][3] = v; continue; }
      v = tr_add<XOR2>(lv[h][3], v, b1);
      if (!(n & 16)) { lv[h][4] = v; continue; }
      v = tr_add<XOR1>(lv[h][4], v, b0);
      lv[h][5] = v;
    }
    __builtin_amdgcn_sched_barrier(0);
  }
  constexpr float C2 = 0.0625f * 1.4426950408889634f;
  const int pi = (lane & 32) | (__builtin_bitreverse32(lane & 31) >> 27);
#pragma unroll
  for (int h = 0; h < NH; ++h) {
    const float s = lv[h][5] * C2;
    const float m = wave_max(s);
    const float p = __builtin_amdgcn_exp2f(s - m);
    const float l = wave_sum(p);
    pbuf[h * 64 + pi] = p;
    if (lane == 0) { ml[2 * h] = m; ml[2 * h + 1] = l; }
  }
  __builtin_amdgcn_fence(__ATOMIC_RELEASE, "wavefront");
  __builtin_amdgcn_wave_barrier();
  f32x2 acc[NH][4];
#pragma unroll
  for (int h = 0; h < NH; ++h)
#pragma unroll
    for (int k = 0; k < 4; ++k) acc[h][k] = f32x2{0.f, 0.f};
  f32x4 pv[NH];
#pragma unroll
  for (int i = 0; i < 32; ++i) {
    const int n = 32 + i;
    if ((i & 3) == 0) {
#pragma unroll
      for (int h = 0; h < NH; ++h) pv[h] = *(const LAS f32x4*)(pbuf + h * 64 + (lane & 32) + i);
    }
    const v4u cur = a[n % W];
    f32x2 vf[4];
#pragma unroll
    for (int k = 0; k < 4; ++k) vf[k] = f32x2{bflo(cur[k]), bfhi(cur[k])};
#pragma unroll
    for (int h = 0; h < NH; ++h) {
      const float p = pv[h][i & 3];
      const f32x2 pp = {p, p};
#pragma unroll
      for (int k = 0; k < 4; ++k) acc[h][k] = __builtin_elementwise_fma(pp, vf[k], acc[h][k]);
      asm volatile("" : "+v"(acc[h][0]), "+v"(acc[h][1]), "+v"(acc[h][2]), "+v"(acc[h][3]));
    }
    __builtin_amdgcn_sched_barrier(0);
    if (n + W < 64) a[n % W] = ldkv(vp, voff, (n + W - 32) * STR);
    __builtin_amdgcn_sched_barrier(0);
  }
#pragma unroll
  for (int h = 0; h < NH; ++h) {
    f32x4 o;
    o[0] = swap32_add(acc[h][0][0], acc[h][2][0]);
    o[1] = swap32_add(acc[h][0][1], acc[h][2][1]);
    o[2] = swap32_add(acc[h][1][0], acc[h][3][0]);
    o[3] = swap32_add(acc[h][1][1], acc[h][3][1]);
    *(LAS f32x4*)(obuf + h * 256 + 8 * (lane & 31) + 4 * (lane >> 5)) = o;
  }
}

__global__ void __launch_bounds__(768) k_attn(const unsigned short* kA, const unsigned short* vA, const unsigned short* kB, const unsigned short* vB, unsigned char* ws) {
  __shared__ __attribute__((aligned(16))) float lds[12 * 128 + 16 * 256 + 16 * 2 + 20480];
  LAS float* L = (LAS float*)lds;
  const int b = blockIdx.x, t = b & 7, j = b >> 3, tid = threadIdx.x, lane = tid & 63;
  const int w = __builtin_amdgcn_readfirstlane(tid >> 6), u = w >> 2, sub = w & 3;
  const GAS v4u* qg = (const GAS v4u*)(ws + WS_Q) + t * 128;
  LAS float* pbuf = L + w * 128;
  LAS float* OB = L + 12 * 128;
  LAS float* ML = OB + 16 * 256;
  const size_t keyoff = (size_t)(256 * j + 64 * sub) * 256;
  const int voff = lane * 16;
  if (u < 2) {
    const size_t hoff = (size_t)(2 * t + u) * SK * 256;
    v4u q[1] = {qg[u * 32 + (lane & 31)]};
    stream_wave<1>(mkrsrc(kA + hoff + keyoff, 32768), mkrsrc(vA + hoff + keyoff, 32768), voff, q, pbuf, OB + (u * 4 + sub) * 256, ML + (u * 4 + sub) * 2, lane);
  } else {
    const size_t hoff = (size_t)t * SK * 256;
    v4u q[2] = {qg[64 + (lane & 31)], qg[96 + (lane & 31)]};
    stream_wave<2>(mkrsrc(kB + hoff + keyoff, 32768), mkrsrc(vB + hoff + keyoff, 32768), voff, q, pbuf, OB + (8 + 2 * sub) * 256, ML + (8 + 2 * sub) * 2, lane);
  }
  __syncthreads();
  if (tid < 256) {
    const int qh = tid >> 6, d4 = tid & 63;
    int slot[4];
#pragma unroll
    for (int s = 0; s < 4; ++s) slot[s] = qh < 2 ? qh * 4 + s : 8 + 2 * s + (qh - 2);
    float m[4], M = -INFINITY;
#pragma unroll
    for (int s = 0; s < 4; ++s) { m[s] = ML[slot[s] * 2]; M = fmaxf(M, m[s]); }
    float Ls = 0.f; f32x4 o = {0.f, 0.f, 0.f, 0.f};
#pragma unroll
    for (int s = 0; s < 4; ++s) {
      const float wt = __builtin_amdgcn_exp2f(m[s] - M);
      Ls = fmaf(wt, ML[slot[s] * 2 + 1], Ls);
      o += wt * *(const LAS f32x4*)(OB + slot[s] * 256 + 4 * d4);
    }
    const int c = qh * 8 + (d4 >> 3);
    float* part = (float*)(ws + WS_PART) + (((size_t)t * 32 + c) * 32 + j) * 32 + 4 * (d4 & 7);
    *(f32x4*)part = o;
    if (d4 == 0) { float* ml = (float*)(ws + WS_ML) + ((t * 4 + qh) * 32 + j) * 2; ml[0] = M; ml[1] = Ls; }
  }
}

__global__ void __launch_bounds__(256) k_oproj(const unsigned short* woA, const unsigned short* woB, unsigned char* ws) {
  __shared__ __attribute__((aligned(16))) float red[4][448];
  const int b = blockIdx.x, t = b & 7, j = b >> 3, tid = threadIdx.x, lane = tid & 63, w = tid >> 6;
  const unsigned short* wo = (j < 16) ? woA : woB;
  const int row0 = 512 * t + 32 * (j & 15) + 8 * w;
  v4u wv[8];
  const int lc = lane < 56 ? lane : 55;
#pragma unroll
  for (int r = 0; r < 8; ++r) wv[r] = *(const GAS v4u*)(wo + (size_t)(row0 + r) * D + 8 * lc);
  const int qh = j >> 3, pr = lane >> 1;
  const float* ml = (const float*)(ws + WS_ML) + ((t * 4 + qh) * 32 + pr) * 2;
  const float m = ml[0], l = ml[1];
  f32x4 o = *(const f32x4*)((const float*)(ws + WS_PART) + (((size_t)t * 32 + j) * 32 + pr) * 32 + 8 * w + 4 * (lane & 1));
  const float M = wave_max(m);
  const float wt = __builtin_amdgcn_exp2f(m - M);
  const float Ls = wave_sum(wt * l) * 0.5f;
  const float inv = 1.0f / Ls;
  float of[8];
#pragma unroll
  for (int k = 0; k < 4; ++k) {
    float v = o[k] * wt;
    v += __shfl_xor(v, 2); v += __shfl_xor(v, 4); v += __shfl_xor(v, 8); v += __shfl_xor(v, 16); v += __shfl_xor(v, 32);
    v = rbf(v * inv);
    of[k] = __shfl(v, 0); of[k + 4] = __shfl(v, 1);
  }
  float acc[8];
#pragma unroll
  for (int k = 0; k < 8; ++k) acc[k] = 0.f;
#pragma unroll
  for (int r = 0; r < 8; ++r)
#pragma unroll
    for (int k = 0; k < 4; ++k) { acc[2 * k] = fmaf(of[r], bflo(wv[r][k]), acc[2 * k]); acc[2 * k + 1] = fmaf(of[r], bfhi(wv[r][k]), acc[2 * k + 1]); }
  if (lane < 56) {
#pragma unroll
    for (int k = 0; k < 8; ++k) red[w][8 * lane + k] = acc[k];
  }
  __syncthreads();
  for (int c = tid; c < D; c += 256) ((float*)(ws + WS_Y))[(size_t)b * D + c] = (red[0][c] + red[1][c]) + (red[2][c] + red[3][c]);
}

__global__ void __launch_bounds__(256) k_final(const unsigned short* x, const unsigned short* gw, const unsigned char* ws, unsigned short* out) {
  __shared__ float red[2][4][64];
  const int tid = threadIdx.x, lane = tid & 63, w = tid >> 6, c = blockIdx.x * 64 + lane;
  const float* y = (const float*)(ws + WS_Y);
  float sa = 0.f, sb = 0.f;
#pragma unroll 8
  for (int i = 0; i < 32; ++i) { sa += y[(size_t)(w * 32 + i) * D + c]; sb += y[(size_t)(128 + w * 32 + i) * D + c]; }
  red[0][w][lane] = sa; red[1][w][lane] = sb;
  float gs = 0.f;
#pragma unroll
  for (int i = 0; i < 7; ++i) gs += rbf(bf2f(x[lane + 64 * i]) * bf2f(gw[lane + 64 * i]));
  gs = rbf(wave_sum(gs));
  const float g = rbf(1.0f / (1.0f + __expf(-gs)));
  __syncthreads();
  if (w == 0) {
    const float a = rbf((red[0][0][lane] + red[0][1][lane]) + (red[0][2][lane] + red[0][3][lane]));
    const float bb = rbf((red[1][0][lane] + red[1][1][lane]) + (red[1][2][lane] + red[1][3][lane]));
    out[c] = f2bf(rbf(g * a) + rbf(rbf(1.0f - g) * bb));
  }
}

extern "C" void kernel_launch(void* const* d_in, const int* in_sizes, int n_in, void* d_out, int out_size, void* d_ws, size_t ws_size, hipStream_t stream) {
  const unsigned short* x = (const unsigned short*)d_in[0];
  unsigned char* ws = (unsigned char*)d_ws;
  k_qproj<<<256, 256, 0, stream>>>(x, (const unsigned short*)d_in[3], (const unsigned short*)d_in[8], ws);
  k_attn<<<256, 768, 0, stream>>>((const unsigned short*)d_in[1], (const unsigned short*)d_in[2], (const unsigned short*)d_in[6], (const unsigned short*)d_in[7], ws);
  k_oproj<<<256, 256, 0, stream>>>((const unsigned short*)d_in[4], (const unsigned short*)d_in[9], ws);
  k_final<<<7, 256, 0, stream>>>(x, (const unsigned short*)d_in[10], ws, (unsigned short*)d_out);
}
```

```cpp
#include <hip/hip_runtime.h>
#include <hip/hip_bf16.h>
#include <stdint.h>

#define GAS __attribute__((address_space(1)))
#define LAS __attribute__((address_space(3)))
typedef unsigned v4u __attribute__((ext_vector_type(4)));
typedef unsigned v2u __attribute__((ext_vector_type(2)));
typedef float f32x4 __attribute__((ext_vector_type(4)));
typedef float f32x2 __attribute__((ext_vector_type(2)));
typedef __bf16 bf16x2 __attribute__((ext_vector_type(2)));
typedef __amdgpu_buffer_rsrc_t rsrc_t;

#define WIN 20
#define PAT 3
#define DIAG 0
#define WO_LATE 1
#define PRE 2
#define QPUSH 0
#define ABL 0
#define TSLEEP 0
#define TMASK 1
#define QDELAY 100
#define AUXWO 2
#define AUXWQ 2
#define VLSLEEP 8
#define HPRIO 3
#define ODSLEEP 1
#define MLSLEEP 4
#define MLAUX 16
#define PX 1
#define QSLEEP 16
constexpr int AUX_NT = 2, AUX_SC1 = 16;
constexpr int D = 448, HD = 4096, SK = 8192;
constexpr unsigned SENT = 0xFFFFFFFFu;
constexpr unsigned SPIN_CAP = 1u << 18;

__host__ __device__ constexpr int step_off(int i) {
  return PAT == 0 ? i * 1024 : PAT == 1 ? i * 131072 : PAT == 2 ? i * 4096 : PAT == 3 ? (i >> 2) * 524288 + (i & 3) * 1024 : (i >> 1) * 262144 + (i & 1) * 1024;
}
__device__ __forceinline__ int wave_off(int j, int sub) {
  return PAT == 0 ? (j * 4 + sub) * 32768 : PAT == 1 ? (j * 4 + sub) * 1024 : PAT == 2 ? j * 131072 + sub * 1024 : PAT == 3 ? (j * 4 + sub) * 4096 : (j * 4 + sub) * 2048;
}

__device__ __forceinline__ float dot2(unsigned k, unsigned q, float acc) {
  return __builtin_amdgcn_fdot2_f32_bf16(__builtin_bit_cast(bf16x2, k), __builtin_bit_cast(bf16x2, q), acc, false);
}
__device__ __forceinline__ float bflo(unsigned u) { return __builtin_bit_cast(float, u << 16); }
__device__ __forceinline__ float bfhi(unsigned u) { return __builtin_bit_cast(float, u & 0xffff0000u); }
__device__ __forceinline__ float bf2f(unsigned short b) { return __builtin_bit_cast(float, (unsigned)b << 16); }
__device__ __forceinline__ unsigned f2bf(float f) { return __builtin_bit_cast(unsigned short, (__bf16)f); }
__device__ __forceinline__ float rbf(float f) { return (float)(__bf16)f; }
template <int CTRL> __device__ __forceinline__ float dpp(float x) {
  return __builtin_bit_cast(float, __builtin_amdgcn_mov_dpp(__builtin_bit_cast(int, x), CTRL, 0xf, 0xf, true));
}
constexpr int XOR1 = 0xB1, XOR2 = 0x4E, XOR7 = 0x141, XOR8 = 0x128, ROR4 = 0x124;
__device__ __forceinline__ rsrc_t mkrsrc(const void* p, unsigned bytes) { return __builtin_amdgcn_make_buffer_rsrc((void*)p, 0, bytes, 0x00020000); }
__device__ __forceinline__ v4u ldkv(rsrc_t r, int voff, int soff) { return __builtin_amdgcn_raw_buffer_load_b128(r, voff, soff, AUX_NT); }
__device__ __forceinline__ float swap16_add(float a, float b) {
  auto s = __builtin_amdgcn_permlane16_swap(__float_as_uint(a), __float_as_uint(b), false, false);
  return __uint_as_float(s[0]) + __uint_as_float(s[1]);
}
__device__ __forceinline__ float swap32_add(float a, float b) {
  auto s = __builtin_amdgcn_permlane32_swap(__float_as_uint(a), __float_as_uint(b), false, false);
  return __uint_as_float(s[0]) + __uint_as_float(s[1]);
}
template <int CTRL> __device__ __forceinline__ float tr_add(float a, float b, bool bit) {
  const float keep = bit ? b : a, send = bit ? a : b;
  return keep + dpp<CTRL>(send);
}
__device__ __forceinline__ float wave_max(float x) {
  x = fmaxf(x, dpp<XOR1>(x)); x = fmaxf(x, dpp<XOR2>(x)); x = fmaxf(x, dpp<XOR7>(x)); x = fmaxf(x, dpp<XOR8>(x));
  auto s = __builtin_amdgcn_permlane16_swap(__float_as_uint(x), __float_as_uint(x), false, false);
  x = fmaxf(__uint_as_float(s[0]), __uint_as_float(s[1]));
  auto t = __builtin_amdgcn_permlane32_swap(__float_as_uint(x), __float_as_uint(x), false, false);
  return fmaxf(__uint_as_float(t[0]), __uint_as_float(t[1]));
}
__device__ __forceinline__ float wave_sum(float x) {
  x += dpp<XOR1>(x); x += dpp<XOR2>(x); x += dpp<XOR7>(x); x += dpp<XOR8>(x);
  x = swap16_add(x, x);
  return swap32_add(x, x);
}
__device__ __forceinline__ float sum_keep1(float x) {
  x += dpp<XOR2>(x); x += dpp<ROR4>(x); x += dpp<XOR8>(x);
  x = swap16_add(x, x);
  return swap32_add(x, x);
}
__device__ __forceinline__ float sum_keep2(float x) {
  x += dpp<ROR4>(x); x += dpp<XOR8>(x);
  x = swap16_add(x, x);
  return swap32_add(x, x);
}
#define WG_BARRIER() asm volatile("s_waitcnt lgkmcnt(0)\n\ts_barrier" ::: "memory")
#define DRAIN() asm volatile("s_waitcnt vmcnt(0)" ::: "memory")
#define SB() __builtin_amdgcn_sched_barrier(0)
__device__ __forceinline__ unsigned nosent(float f) { const unsigned u = __float_as_uint(f); return u == SENT ? 0x7FC00000u : u; }

constexpr int NRED = 112;
constexpr int WS_EPOCH = 0;
constexpr int WS_CF = 1024;
constexpr int WS_QG = 4096;
constexpr int PART_STR = 4096 + 256;
constexpr int WS_PART = WS_QG + 256 * 4096;
constexpr int WS_Y = WS_PART + 256 * PART_STR;
constexpr int WS_END = WS_Y + NRED * 4096;
constexpr int WS_DIAG = 4 << 20;

constexpr int L_Q = 0;
constexpr int L_P = L_Q + 512;
constexpr int L_O = L_P + 12 * 128;
constexpr int L_ML = L_O + 16 * 256;
constexpr int L_RED = L_ML + 32;
constexpr int L_CTL = L_RED + 128;
constexpr int C_QP = 0, C_QR = 1, C_CL = 2, C_OK = 3, C_VL = 4, C_KD = 5  , C_OD = 8  , C_N = 16;
constexpr int L_END = L_CTL + C_N;

#define STAMP_DECL
#define STAMP(i) do { } while (0)
#define STAMP_DUMP() do { } while (0)

#define CBAR() asm volatile("" ::: "memory")
__device__ __forceinline__ unsigned lds_arrive(LAS unsigned* p, int lane) {
  CBAR();
  unsigned old = 0;
  if (lane == 0) old = __hip_atomic_fetch_add(p, 1u, __ATOMIC_RELAXED, __HIP_MEMORY_SCOPE_WORKGROUP);
  old = __builtin_amdgcn_readfirstlane(old);
  CBAR();
  return old;
}
template <int SLEEP> __device__ __forceinline__ void lds_wait(LAS unsigned* p, unsigned want) {
#pragma nounroll
  for (unsigned spins = 0; spins < SPIN_CAP * 16; ++spins) {
    CBAR();
    if (__builtin_amdgcn_readfirstlane(*(volatile LAS unsigned*)p) >= want) break;
    __builtin_amdgcn_s_sleep(SLEEP);
  }
  CBAR();
}
__device__ __forceinline__ unsigned umax4(v4u v) { return max(max(v[0], v[1]), max(v[2], v[3])); }

typedef unsigned long long u64;
__device__ __forceinline__ void gather4(v4u& a, v4u& b, v4u& c, v4u& d, v4u srd, int off) {
  u64 m0 = ~0ull, m1 = ~0ull, m2 = ~0ull, m3 = ~0ull;
  a = b = c = d = v4u{SENT, SENT, SENT, SENT};
#pragma nounroll
  for (unsigned spins = 0; spins < SPIN_CAP; ++spins) {
    asm volatile("s_mov_b64 exec, %5\n\t"
                 "buffer_load_dwordx4 %0, %4, %9, 0 offen sc1\n\t"
                 "s_mov_b64 exec, %6\n\t"
                 "buffer_load_dwordx4 %1, %4, %9, 0 offen offset:1024 sc1\n\t"
                 "s_mov_b64 exec, %7\n\t"
                 "buffer_load_dwordx4 %2, %4, %9, 0 offen offset:2048 sc1\n\t"
                 "s_mov_b64 exec, %8\n\t"
                 "buffer_load_dwordx4 %3, %4, %9, 0 offen offset:3072 sc1\n\t"
                 "s_mov_b64 exec, -1\n\t"
                 "s_waitcnt vmcnt(0)"
                 : "+v"(a), "+v"(b), "+v"(c), "+v"(d)
                 : "v"(off), "s"(m0), "s"(m1), "s"(m2), "s"(m3), "s"(srd)
                 : "memory");
    const u64 n0 = __ballot(umax4(a) == SENT), n1 = __ballot(umax4(b) == SENT), n2 = __ballot(umax4(c) == SENT), n3 = __ballot(umax4(d) == SENT);
    if ((n0 | n1 | n2 | n3) == 0) break;
    if (TMASK) { m0 = n0; m1 = n1; m2 = n2; m3 = n3; }
    if (TSLEEP) __builtin_amdgcn_s_sleep(TSLEEP);
  }
}

template <int NH>
__device__ __forceinline__ void stream_wave(rsrc_t kp, rsrc_t vp, rsrc_t rws, int voff, LAS float* L, int sw, int t, int j, int lane, unsigned char* ws) {
  STAMP_DECL;
  STAMP(0);
  constexpr int W = WIN;
  const int u = sw >> 2, sub = sw & 3;
  LAS unsigned* ctl = (LAS unsigned*)(L + L_CTL);
  LAS float* pbuf = L + L_P + sw * 128;
  v4u a[W];
#pragma unroll
  for (int n = 0; n < W; ++n) {
    a[n] = n < 32 ? ldkv(kp, voff, step_off(n)) : ldkv(vp, voff, step_off(n - 32)); SB();
    if (n == PRE - 1) WG_BARRIER();
  }
  STAMP(1);
  lds_wait<2>(ctl + C_QR, 4);
  STAMP(2);
  v4u q[NH];
#pragma unroll
  for (int h = 0; h < NH; ++h) q[h] = ((const LAS v4u*)(L + L_Q))[(u + h) * 32 + (lane & 31)];
  float lv[NH][6];
  const bool b3 = lane & 8, b2 = lane & 4, b1 = lane & 2, b0 = lane & 1;
#pragma unroll
  for (int n = 0; n < 32; ++n) {
    const v4u cur = a[n % W];
    float x[NH];
#pragma unroll
    for (int h = 0; h < NH; ++h) {
      float d;
      if (ABL & 32) d = __uint_as_float(cur[0] ^ cur[3]);
      else { d = dot2(cur[0], q[h][0], 0.f);
      d = dot2(cur[1], q[h][1], d); d = dot2(cur[2], q[h][2], d); d = dot2(cur[3], q[h][3], d); }
      x[h] = d;
      asm volatile("" : "+v"(x[h]));
    }
    SB();
    if (n + W < 64) a[n % W] = n + W < 32 ? ldkv(kp, voff, step_off(n + W)) : ldkv(vp, voff, step_off(n + W - 32));
#pragma unroll
    for (int h = 0; h < NH; ++h) {
      float v = x[h];
      if (ABL & 32) { lv[h][5] = n ? lv[h][5] + v : v; continue; }
      if (!(n & 1)) { lv[h][0] = v; continue; }
      v = swap16_add(lv[h][0], v);
      if (!(n & 2)) { lv[h][1] = v; continue; }
      v = tr_add<XOR8>(lv[h][1], v, b3);
      if (!(n & 4)) { lv[h][2] = v; continue; }
      v = tr_add<XOR7>(lv[h][2], v, b2);
      if (!(n & 8)) { lv[h][3] = v; continue; }
      v = tr_add<XOR2>(lv[h][3], v, b1);
      if (!(n & 16)) { lv[h][4] = v; continue; }
      v = tr_add<XOR1>(lv[h][4], v, b0);
      lv[h][5] = v;
    }
    SB();
  }
  constexpr float C2 = 0.0625f * 1.4426950408889634f;
  const int pi = (lane & 32) | (__builtin_bitreverse32(lane & 31) >> 27);
  float mw[NH];
#pragma unroll
  for (int h = 0; h < NH; ++h) {
    const float s = lv[h][5] * C2;
    const float m = wave_max(s);
    const float p = __builtin_amdgcn_exp2f(s - m);
    const float l = wave_sum(p);
    pbuf[h * 64 + pi] = p;
    mw[h] = m;
    if (lane == 0) { L[L_ML + (u + h) * 8 + sub] = m; L[L_ML + (u + h) * 8 + 4 + sub] = l; }
  }
  CBAR();
  if (lane == 0) __hip_atomic_fetch_add(ctl + C_KD + u, 1u, __ATOMIC_RELAXED, __HIP_MEMORY_SCOPE_WORKGROUP);
  CBAR();
  STAMP(3);
  f32x2 acc[NH][4];
#pragma unroll
  for (int h = 0; h < NH; ++h)
#pragma unroll
    for (int k = 0; k < 4; ++k) acc[h][k] = f32x2{0.f, 0.f};
  f32x4 pv[NH];
#pragma unroll
  for (int i = 0; i < 32; ++i) {
    const int n = 32 + i;
    if (i == 28) {
      lds_wait<1>(ctl + C_KD + u, 4);
#pragma unroll
      for (int h = 0; h < NH; ++h) {
        const f32x4 m4 = *(const LAS f32x4*)(L + L_ML + (u + h) * 8);
        mw[h] = __builtin_amdgcn_exp2f(mw[h] - fmaxf(fmaxf(m4[0], m4[1]), fmaxf(m4[2], m4[3])));
        asm volatile("" : "+v"(mw[h]));
      }
    }
    if ((i & 3) == 0) {
#pragma unroll
      for (int h = 0; h < NH; ++h) pv[h] = *(const LAS f32x4*)(pbuf + h * 64 + (lane & 32) + i);
    }
    const v4u cur = a[n % W];
    f32x2 vf[4];
#pragma unroll
    for (int k = 0; k < 4; ++k) vf[k] = f32x2{bflo(cur[k]), bfhi(cur[k])};
#pragma unroll
    for (int h = 0; h < NH; ++h) {
      const float p = pv[h][i & 3];
      const f32x2 pp = {p, p};
      if (ABL & 16) acc[h][0][0] += __uint_as_float(cur[0] ^ cur[3]) * p; else
#pragma unroll
      for (int k = 0; k < 4; ++k) acc[h][k] = __builtin_elementwise_fma(pp, vf[k], acc[h][k]);
      asm volatile("" : "+v"(acc[h][0]), "+v"(acc[h][1]), "+v"(acc[h][2]), "+v"(acc[h][3]));
    }
    SB();
    if (n + W < 64) a[n % W] = ldkv(vp, voff, step_off(n + W - 32));
    if (NH == 2 && n + W == 63) { if (lane == 0) __hip_atomic_fetch_add(ctl + C_VL, 1u, __ATOMIC_RELAXED, __HIP_MEMORY_SCOPE_WORKGROUP); }
    SB();
  }
  STAMP(4);
#pragma unroll
  for (int h = 0; h < NH; ++h) {
    f32x4 o;
    o[0] = swap32_add(acc[h][0][0], acc[h][2][0]);
    o[1] = swap32_add(acc[h][0][1], acc[h][2][1]);
    o[2] = swap32_add(acc[h][1][0], acc[h][3][0]);
    o[3] = swap32_add(acc[h][1][1], acc[h][3][1]);
    *(LAS f32x4*)(L + L_O + ((u + h) * 4 + sub) * 256 + 8 * (lane & 31) + 4 * (lane >> 5)) = o * mw[h];
  }
  if (lds_arrive(ctl + C_OD + u, lane) == 3) {
#pragma unroll
    for (int h = 0; h < NH; ++h) {
      const LAS f32x4* O = (const LAS f32x4*)(L + L_O + (u + h) * 1024) + lane;
      const f32x4 o = (O[0] + O[64]) + (O[128] + O[192]);
      const v4u ov = {nosent(o[0]), nosent(o[1]), nosent(o[2]), nosent(o[3])};
      __builtin_amdgcn_raw_buffer_store_b128(ov, rws, WS_PART + (t * 32 + (u + h) * 8 + (lane >> 3)) * PART_STR + j * 128 + (lane & 7) * 16, 0, AUX_SC1);
    }
  }
  STAMP(5);
  STAMP_DUMP();
}

__global__ void __launch_bounds__(1024) k_fused(const unsigned short* x, const unsigned short* kA, const unsigned short* vA, const unsigned short* wqA, const unsigned short* woA,
                                               const unsigned short* kB, const unsigned short* vB, const unsigned short* wqB, const unsigned short* woB,
                                               const unsigned short* gw, unsigned short* out, unsigned char* ws) {
  __shared__ __attribute__((aligned(16))) float lds[L_END];
  LAS float* L = (LAS float*)lds;
  const int b = blockIdx.x, t = b & 7, j = b >> 3, tid = threadIdx.x, lane = tid & 63;
  const int w = __builtin_amdgcn_readfirstlane(tid >> 6);
  const rsrc_t rws = mkrsrc(ws, WS_END);
  if (w >= 4) {
    const int sw = w - 4, u = sw >> 2, sub = sw & 3;
    const int voff = lane * 16 + wave_off(j, sub);
    if (u < 2) {
      const size_t hoff = (size_t)(2 * t + u) * SK * 256;
      stream_wave<1>(mkrsrc(kA + hoff, 4 << 20), mkrsrc(vA + hoff, 4 << 20), rws, voff, L, sw, t, j, lane, ws);
    } else {
      const size_t hoff = (size_t)t * SK * 256;
      stream_wave<2>(mkrsrc(kB + hoff, 4 << 20), mkrsrc(vB + hoff, 4 << 20), rws, voff, L, sw, t, j, lane, ws);
    }
    return;
  }
  STAMP_DECL;
  STAMP(0);
  __builtin_amdgcn_s_setprio(3);
  LAS unsigned* ctl = (LAS unsigned*)(L + L_CTL);
  const v4u srd = {(unsigned)(u64)ws, (unsigned)((u64)ws >> 32) & 0xffffu, (unsigned)WS_END, 0x00020000u};
  const int tp = t ^ PX;
  const int me = tp * 32 + j;
  const int slice = 512 * tp + 32 * (j & 15);
  unsigned tag;
  {
    const rsrc_t rwq = mkrsrc(j < 16 ? wqA : wqB, (ABL & 4) ? 0 : D * HD * 2);
    const int r0 = w * 112 + (lane >> 2);
    v4u wv[7];
#pragma unroll
    for (int i = 0; i < 7; ++i) wv[i] = __builtin_amdgcn_raw_buffer_load_b128(rwq, (r0 * HD + slice + 8 * (lane & 3)) * 2, i * 16 * HD * 2, AUXWQ);
    unsigned xi[7];
    const rsrc_t rx = mkrsrc(x, D * 2);
#pragma unroll
    for (int i = 0; i < 7; ++i) xi[i] = __builtin_amdgcn_raw_buffer_load_b32(rx, (r0 >> 1) * 4, i * 32, 0);
    tag = __builtin_amdgcn_raw_buffer_load_b32(rws, WS_EPOCH, 0, AUX_SC1);
    if (!(ABL & 2)) {
      const v4u s4 = {SENT, SENT, SENT, SENT};
      __builtin_amdgcn_raw_buffer_store_b128(s4, rws, WS_PART + me * PART_STR + tid * 16, 0, AUX_SC1);
      __builtin_amdgcn_raw_buffer_store_b128(s4, rws, tid < 16 ? WS_PART + me * PART_STR + 4096 + tid * 16 : WS_END, 0, AUX_SC1);
      __builtin_amdgcn_raw_buffer_store_b128(s4, rws, WS_Y + b * 4096 + tid * 16, 0, AUX_SC1);
    }
    if (tid < C_N) ctl[tid] = 0;
    SB();
    WG_BARRIER();
    SB();
#pragma unroll
    for (int i = 0; i < 7; ++i) asm volatile("" : "+v"(wv[i]), "+v"(xi[i]));
    f32x2 acc[4];
#pragma unroll
    for (int k = 0; k < 4; ++k) acc[k] = f32x2{0.f, 0.f};
#pragma unroll
    for (int i = 0; i < 7; ++i) {
      const float xv = (lane & 4) ? bfhi(xi[i]) : bflo(xi[i]);
#pragma unroll
      for (int k = 0; k < 4; ++k) acc[k] = __builtin_elementwise_fma(f32x2{xv, xv}, f32x2{bflo(wv[i][k]), bfhi(wv[i][k])}, acc[k]);
    }
    const float c0 = swap32_add(acc[0][0], acc[2][0]), c1 = swap32_add(acc[0][1], acc[2][1]), c2 = swap32_add(acc[1][0], acc[3][0]), c3 = swap32_add(acc[1][1], acc[3][1]);
    float e0 = swap16_add(c0, c2), e1 = swap16_add(c1, c3);
    e0 += dpp<ROR4>(e0); e1 += dpp<ROR4>(e1);
    e0 += dpp<XOR8>(e0); e1 += dpp<XOR8>(e1);
    if ((lane & 12) == 0) *(LAS f32x2*)(L + L_RED + w * 32 + 8 * (lane & 3) + 4 * (lane >> 5) + 2 * ((lane >> 4) & 1)) = f32x2{e0, e1};
    tag += 1u;
    DRAIN();
    if (lds_arrive(ctl + C_QP, lane) == 3) {
      const LAS f32x4* R = (const LAS f32x4*)(L + L_RED) + (lane & 7);
      const f32x4 s = (R[0] + R[8]) + (R[16] + R[24]);
      const v4u gq = {f2bf(s[0]) | (f2bf(s[1]) << 16), tag, f2bf(s[2]) | (f2bf(s[3]) << 16), tag};
      if (lane < 8) __builtin_amdgcn_raw_buffer_store_b128(gq, rws, WS_QG + me * 128 + lane * 16, 0, AUX_SC1);
      if (lane == 0) __builtin_amdgcn_raw_buffer_store_b32(tag, rws, WS_CF + b * 4, 0, AUX_SC1);
    }
  }
  STAMP(1);
  {
    v4u g = {0u, 0u, 0u, 0u};
    bool ok = false;
    if (QDELAY) __builtin_amdgcn_s_sleep(QDELAY);
#pragma nounroll
    for (unsigned spins = 0; spins < ((ABL & 1) ? 0 : SPIN_CAP); ++spins) {
      CBAR();
      if (!ok) g = __builtin_amdgcn_raw_buffer_load_b128(rws, WS_QG + (QPUSH ? t * 32 + j : t) * 4096 + tid * 16, 0, AUX_SC1);
      ok = g[1] == tag && g[3] == tag;
      if (__all(ok)) break;
      __builtin_amdgcn_s_sleep(QSLEEP);
    }
    *(LAS v2u*)(L + L_Q + tid * 2) = v2u{g[0], g[2]};
    lds_arrive(ctl + C_QR, lane);
  }
  STAMP(2);
  if (ABL & 8) { STAMP_DUMP(); return; }
  float gate = 0.f;
  if (b < NRED && w == 0) {
    float gs = 0.f;
#pragma unroll
    for (int i = 0; i < 7; ++i) gs += rbf(bf2f(x[lane + 64 * i]) * bf2f(gw[lane + 64 * i]));
    gs = rbf(wave_sum(gs));
    gate = rbf(1.0f / (1.0f + __expf(-gs)));
    asm volatile("" : "+v"(gate));
  }
  const rsrc_t rwo = mkrsrc(j < 16 ? woA : woB, (ABL & 64) ? 0 : HD * D * 2);
  const int lc = lane < 56 ? lane : 55;
  unsigned wo[32];
  const int cq = j >> 3, cu = cq < 2 ? cq : 2;
  __builtin_amdgcn_s_setprio(HPRIO);
  {
    lds_wait<VLSLEEP>(ctl + C_KD + (w < 2 ? w : 2), 4);
    const f32x4 m4 = *(const LAS f32x4*)(L + L_ML + w * 8), l4 = *(const LAS f32x4*)(L + L_ML + w * 8 + 4);
    const float M = fmaxf(fmaxf(m4[0], m4[1]), fmaxf(m4[2], m4[3]));
    float Ls = 0.f;
#pragma unroll
    for (int s = 0; s < 4; ++s) Ls = fmaf(__builtin_amdgcn_exp2f(m4[s] - M), l4[s], Ls);
    if (lane < 8 && !(ABL & 32768)) __builtin_amdgcn_raw_buffer_store_b64(v2u{nosent(M), nosent(Ls)}, rws, WS_PART + (t * 32 + w * 8 + lane) * PART_STR + 4096 + j * 8, 0, AUX_SC1);
  }
  lds_wait<VLSLEEP>(ctl + C_VL, 4);
  if (ABL & 128) { STAMP_DUMP(); return; }
  float wn[4];
  {
    v2u mlv = (ABL & (2048 | 16384)) ? v2u{0u, 0x3f800000u} : __builtin_amdgcn_raw_buffer_load_b64(rws, ((ABL & 65536) ? WS_QG + 65536 + me * 2048: WS_PART + me * PART_STR + 4096) + (lane & 31) * 8, 0, MLAUX);
    v2u cf = (ABL & (2048 | 8192)) ? v2u{tag, tag} : __builtin_amdgcn_raw_buffer_load_b64(rws, WS_CF + lc * 8, 0, AUX_SC1);
#pragma unroll
    for (int r = 0; r < 32; ++r) wo[r] = (ABL & 1024) ? r * lane : __builtin_amdgcn_raw_buffer_load_b32(rwo, (slice * D + 112 * w + 2 * lc) * 2, r * D * 2, AUXWO);
    if (!(ABL & 4096) && !__all(mlv[0] != SENT && mlv[1] != SENT && cf[0] == tag && cf[1] == tag)) {
#pragma nounroll
      for (unsigned spins = 0; spins < SPIN_CAP; ++spins) {
        CBAR();
        mlv = __builtin_amdgcn_raw_buffer_load_b64(rws, WS_PART + me * PART_STR + 4096 + (lane & 31) * 8, 0, AUX_SC1);
        cf = __builtin_amdgcn_raw_buffer_load_b64(rws, WS_CF + lc * 8, 0, AUX_SC1);
        if (__all(mlv[0] != SENT && mlv[1] != SENT && cf[0] == tag && cf[1] == tag)) break;
        __builtin_amdgcn_s_sleep(MLSLEEP);
      }
    }
    asm volatile("" :: "v"(cf));
    const float m = __uint_as_float(mlv[0]), l = __uint_as_float(mlv[1]);
    const float wt = __builtin_amdgcn_exp2f(m - wave_max(m));
    const float wq = wt * (2.0f / wave_sum(wt * l));
#pragma unroll
    for (int k = 0; k < 4; ++k) wn[k] = __shfl(wq, 8 * k + (lane >> 3));
    asm volatile("" : "+v"(wn[0]), "+v"(wn[1]), "+v"(wn[2]), "+v"(wn[3]));
  }
  STAMP(3);
  unsigned wl[16], wh[16];
#pragma unroll
  for (int r = 0; r < 16; ++r) {
    wl[r] = __builtin_amdgcn_perm(wo[2 * r + 1], wo[2 * r], 0x05040100u);
    wh[r] = __builtin_amdgcn_perm(wo[2 * r + 1], wo[2 * r], 0x07060302u);
    asm volatile("" : "+v"(wl[r]), "+v"(wh[r]));
  }
  if (!PX) lds_wait<ODSLEEP>(ctl + C_OD + cu, 4);
  STAMP(4);
  if (ABL & 256) { STAMP_DUMP(); return; }
  {
    v4u ov[4];
    gather4(ov[0], ov[1], ov[2], ov[3], srd, WS_PART + me * PART_STR + lane * 16);
    STAMP(5);
    f32x4 s = wn[0] * __builtin_bit_cast(f32x4, ov[0]);
#pragma unroll
    for (int k = 1; k < 4; ++k) s = __builtin_elementwise_fma(f32x4{wn[k], wn[k], wn[k], wn[k]}, __builtin_bit_cast(f32x4, ov[k]), s);
    float t0 = swap32_add(s[0], s[2]), t1 = swap32_add(s[1], s[3]);
    t0 = swap16_add(t0, t0); t1 = swap16_add(t1, t1);
    t0 += dpp<XOR8>(t0); t1 += dpp<XOR8>(t1);
    const unsigned pk = f2bf(t0) | (f2bf(t1) << 16);
    float y0a = 0.f, y0b = 0.f, y1a = 0.f, y1b = 0.f;
#pragma unroll
    for (int r = 0; r < 16; r += 2) {
      const unsigned oa = __builtin_amdgcn_readlane(pk, r >> 1), ob = __builtin_amdgcn_readlane(pk, (r >> 1) + 32);
      y0a = dot2(wl[r], oa, y0a); y1a = dot2(wh[r], oa, y1a);
      y0b = dot2(wl[r + 1], ob, y0b); y1b = dot2(wh[r + 1], ob, y1b);
    }
    const int ps = (j >> 4) * 128 + tp * 16 + (j & 15);
    if (lane < 56) __builtin_amdgcn_raw_buffer_store_b64(v2u{nosent(y0a + y0b), nosent(y1a + y1b)}, rws, WS_Y + (28 * w + (lane >> 1)) * 4096 + ps * 16 + (lane & 1) * 8, 0, AUX_SC1);
  }
  STAMP(6);
  if (ABL & 512) { STAMP_DUMP(); return; }
  if (b < NRED && w == 0) {
    v4u y[4];
    gather4(y[0], y[1], y[2], y[3], srd, WS_Y + b * 4096 + lane * 16);
    const f32x4 sa = __builtin_bit_cast(f32x4, y[0]) + __builtin_bit_cast(f32x4, y[1]), sb = __builtin_bit_cast(f32x4, y[2]) + __builtin_bit_cast(f32x4, y[3]);
    const float v0 = swap32_add(sa[0], sb[0]), v1 = swap32_add(sa[1], sb[1]), v2 = swap32_add(sa[2], sb[2]), v3 = swap32_add(sa[3], sb[3]);
    const float z0 = swap16_add(v0, v1), z1 = swap16_add(v2, v3);
    float z = tr_add<XOR8>(z0, z1, lane & 8);
    z += dpp<XOR7>(z); z += dpp<XOR2>(z); z += dpp<XOR1>(z);
    z = rbf(rbf(z) * (lane < 32 ? gate : rbf(1.0f - gate)));
    z = swap32_add(z, z);
    if ((lane & 39) == 0) out[b * 4 + ((lane >> 3) & 1) * 2 + (lane >> 4)] = (unsigned short)f2bf(z);
    if (b == 0 && lane == 0) __builtin_amdgcn_raw_buffer_store_b32(tag, rws, WS_EPOCH, 0, AUX_SC1);
  }
  STAMP(7);
  STAMP_DUMP();
}

extern "C" void kernel_launch(void* const* d_in, const int* in_sizes, int n_in, void* d_out, int out_size, void* d_ws, size_t ws_size, hipStream_t stream) {
  typedef const unsigned short* cp;
  k_fused<<<256, 1024, 0, stream>>>((cp)d_in[0], (cp)d_in[1], (cp)d_in[2], (cp)d_in[3], (cp)d_in[4], (cp)d_in[6], (cp)d_in[7], (cp)d_in[8], (cp)d_in[9], (cp)d_in[10],
                                    (unsigned short*)d_out, (unsigned char*)d_ws);
}
```

```cpp
#include <hip/hip_runtime.h>
#include <hip/hip_bf16.h>
#include <stdint.h>

#define GAS __attribute__((address_space(1)))
#define LAS __attribute__((address_space(3)))
#define CAS __attribute__((address_space(4)))
typedef unsigned v4u __attribute__((ext_vector_type(4)));
typedef unsigned v2u __attribute__((ext_vector_type(2)));
typedef float f32x4 __attribute__((ext_vector_type(4)));
typedef float f32x2 __attribute__((ext_vector_type(2)));
typedef __bf16 bf16x2 __attribute__((ext_vector_type(2)));
typedef __amdgpu_buffer_rsrc_t rsrc_t;
typedef unsigned long long u64;

constexpr int TAU = 215;
constexpr int TAUW = 220;
constexpr int ADP = 5;
constexpr int ASH = 1;
constexpr int WIN = 20;
constexpr int SOFF = 7;
constexpr int GAPS = 3;
constexpr int PRE = 4;
constexpr int WD = 4;
constexpr int WSTEP = 26;
constexpr int NCAL = 12;

constexpr int AUX_NT = 2, AUX_SC1 = 16;
constexpr int D = 448, HD = 4096, SK = 8192;
constexpr unsigned SPIN_CAP = 1u << 18;

__device__ __forceinline__ float dot2(unsigned a, unsigned b, float acc) {
  return __builtin_amdgcn_fdot2_f32_bf16(__builtin_bit_cast(bf16x2, a), __builtin_bit_cast(bf16x2, b), acc, false);
}
__device__ __forceinline__ float bflo(unsigned u) { return __builtin_bit_cast(float, u << 16); }
__device__ __forceinline__ float bfhi(unsigned u) { return __builtin_bit_cast(float, u & 0xffff0000u); }
__device__ __forceinline__ float bf2f(unsigned short b) { return __builtin_bit_cast(float, (unsigned)b << 16); }
__device__ __forceinline__ unsigned short f2bf(float f) { return __builtin_bit_cast(unsigned short, (__bf16)f); }
__device__ __forceinline__ unsigned pk2bf(float lo, float hi) { return __builtin_bit_cast(unsigned, bf16x2{(__bf16)lo, (__bf16)hi}); }
__device__ __forceinline__ float rbf(float f) { return (float)(__bf16)f; }
template <int CTRL> __device__ __forceinline__ float dpp(float x) {
  return __builtin_bit_cast(float, __builtin_amdgcn_mov_dpp(__builtin_bit_cast(int, x), CTRL, 0xf, 0xf, true));
}
constexpr int XOR1 = 0xB1, XOR2 = 0x4E, XOR7 = 0x141, XOR8 = 0x128, ROR4 = 0x124;
__device__ __forceinline__ rsrc_t mkrsrc(const void* p, unsigned bytes) { return __builtin_amdgcn_make_buffer_rsrc((void*)p, 0, bytes, 0x00020000); }
__device__ __forceinline__ v4u mksrd(const void* p, unsigned bytes) { return v4u{(unsigned)(u64)p, (unsigned)((u64)p >> 32) & 0xffffu, bytes, 0x00020000u}; }
__device__ __forceinline__ float swap16_add(float a, float b) {
  auto s = __builtin_amdgcn_permlane16_swap(__float_as_uint(a), __float_as_uint(b), false, false);
  return __uint_as_float(s[0]) + __uint_as_float(s[1]);
}
__device__ __forceinline__ float swap32_add(float a, float b) {
  auto s = __builtin_amdgcn_permlane32_swap(__float_as_uint(a), __float_as_uint(b), false, false);
  return __uint_as_float(s[0]) + __uint_as_float(s[1]);
}
__device__ __forceinline__ float wave_max(float x) {
  x = fmaxf(x, dpp<XOR1>(x)); x = fmaxf(x, dpp<XOR2>(x)); x = fmaxf(x, dpp<XOR7>(x)); x = fmaxf(x, dpp<XOR8>(x));
  auto s = __builtin_amdgcn_permlane16_swap(__float_as_uint(x), __float_as_uint(x), false, false);
  x = fmaxf(__uint_as_float(s[0]), __uint_as_float(s[1]));
  auto t = __builtin_amdgcn_permlane32_swap(__float_as_uint(x), __float_as_uint(x), false, false);
  return fmaxf(__uint_as_float(t[0]), __uint_as_float(t[1]));
}
__device__ __forceinline__ float wave_sum(float x) {
  x += dpp<XOR1>(x); x += dpp<XOR2>(x); x += dpp<XOR7>(x); x += dpp<XOR8>(x);
  x = swap16_add(x, x);
  return swap32_add(x, x);
}
template <int M> __device__ __forceinline__ float put8(float old, float v) {
  return __builtin_bit_cast(float, __builtin_amdgcn_update_dpp(__builtin_bit_cast(int, old), __builtin_bit_cast(int, v), 0xE4, 1 << (M >> 1), (M & 1) ? 0xC : 0x3, false));
}
template <int N> struct IC { static constexpr int v = N; };
template <int I, int N, class F> __device__ __forceinline__ void sfor(F&& f) {
  if constexpr (I < N) { f(IC<I>{}); sfor<I + 1, N>(f); }
}
#define SFOR(n, lo, hi) sfor<lo, hi>([&](auto n##_) __attribute__((always_inline)) { constexpr int n = decltype(n##_)::v;
#define SEND });
#define WG_BARRIER_LDS() asm volatile("s_waitcnt lgkmcnt(0)\n\ts_barrier" ::: "memory")
#define WG_BARRIER() asm volatile("s_barrier" ::: "memory")
#define SB() __builtin_amdgcn_sched_barrier(0)
#define CBAR() asm volatile("" ::: "memory")

constexpr unsigned TM = 1023u;
__device__ __forceinline__ v4u tagfields(u64 tag) { return v4u{(unsigned)tag & TM, (unsigned)(tag >> 16) & TM, (unsigned)(tag >> 32) & TM, (unsigned)(tag >> 48) & TM}; }
__device__ __forceinline__ v4u seal(f32x4 v, v4u tb, unsigned keep = ~TM) {
  const v4u u = __builtin_bit_cast(v4u, v);
  return v4u{(u[0] & keep) | tb[0], (u[1] & keep) | tb[1], (u[2] & keep) | tb[2], (u[3] & keep) | tb[3]};
}
__device__ __forceinline__ bool stale(v4u x) { return ((x[0] | x[1] | x[2] | x[3]) & TM) != 0; }

constexpr int NRED = 112;
constexpr int WS_EPOCH = 0;
constexpr int WS_QG = 4096;
constexpr int WS_ML = WS_QG + 8 * 8192;
constexpr int WS_GP = 256;
constexpr int WS_CAL = 98304;
constexpr int WS_O = 131072;
constexpr int WS_Y = WS_O + 256 * 4096;
constexpr int WS_END = WS_Y + NRED * 4096;
static_assert(WS_ML + 32 * 512 <= WS_CAL && WS_CAL + 32768 <= WS_O && WS_END <= (2 << 20), "hand-off area");
__device__ __forceinline__ int blk(int i, int c) { return (((i >> 1) << 2) | (c << 1) | (i & 1)) * 4096; }

constexpr int L_Q = 0;
constexpr int L_P = L_Q + 512;
constexpr int L_O = L_P + 12 * 128;
constexpr int L_ML = L_O + 16 * 256;
constexpr int L_RED = L_ML + 32;
constexpr int L_Y = L_RED + 256;
constexpr int L_WN = L_Y + 4 * 448;
constexpr int L_CTL = L_WN + 32;
constexpr int C_QP = 0, C_QR = 1, C_SL = 2, C_WN = 3, C_VL = 4, C_KD = 5  , C_OD = 8  , C_GP = 12, C_T0 = 15, C_N = 16;
constexpr int L_END = L_CTL + C_N;

__device__ __forceinline__ int fresh_lane() {
  int l;
  asm volatile("v_mbcnt_lo_u32_b32 %0, -1, 0\n\tv_mbcnt_hi_u32_b32 %0, -1, %0" : "=v"(l));
  return l;
}
__device__ __forceinline__ unsigned lds_arrive(LAS unsigned* p, int lane) {
  CBAR();
  unsigned old = 0;
  if (lane == 0) old = __hip_atomic_fetch_add(p, 1u, __ATOMIC_RELAXED, __HIP_MEMORY_SCOPE_WORKGROUP);
  old = __builtin_amdgcn_readfirstlane(old);
  CBAR();
  return old;
}
__device__ __forceinline__ void lds_signal(LAS unsigned* p, int lane) {
  CBAR();
  if (lane == 0) __hip_atomic_fetch_add(p, 1u, __ATOMIC_RELAXED, __HIP_MEMORY_SCOPE_WORKGROUP);
  CBAR();
}
template <int SLEEP> __device__ __forceinline__ void lds_wait(LAS unsigned* p, unsigned want) {
#pragma nounroll
  for (unsigned spins = 0; spins < SPIN_CAP * 64; ++spins) {
    CBAR();
    if (__builtin_amdgcn_readfirstlane(*(volatile LAS unsigned*)p) >= want) break;
    __builtin_amdgcn_s_sleep(SLEEP);
  }
  CBAR();
}
__device__ __forceinline__ u64 load_tag(const unsigned char* ws) {
  u64 p = (u64)ws + WS_EPOCH;
  asm volatile("" : "+s"(p));
  return *(const CAS u64*)p + 0x0001000100010001ull;
}
__device__ __forceinline__ unsigned char* ws_area(unsigned char* ws0) {
  u64 p = (u64)ws0;
  asm volatile("" : "+s"(p));
  return (unsigned char*)((p + 0x1fffff) & ~0x1fffffull);
}
__device__ __forceinline__ v4u ldsc(rsrc_t r, int off) { return __builtin_amdgcn_raw_buffer_load_b128(r, off, 0, AUX_SC1); }

__device__ __forceinline__ v4u gather1(v4u a, v4u srd, int off, v4u tb) {
  v4u xa = a ^ tb;
  u64 m0 = __ballot(stale(xa));
  if (m0 != 0) {
    u64 go = ~0ull;
    unsigned left = SPIN_CAP;
#pragma nounroll
    do {
      asm volatile("s_mov_b64 exec, %3\n\t"
                   "buffer_load_dwordx4 %0, %4, %5, 0 offen sc1\n\t"
                   "s_mov_b64 exec, -1\n\t"
                   "s_sub_u32 %1, %1, 1\n\t"
                   "s_cselect_b64 %2, 0, %2\n\t"
                   "s_waitcnt vmcnt(0)"
                   : "+v"(a), "+s"(left), "+s"(go) : "s"(m0), "v"(off), "s"(srd) : "memory", "scc");
      xa = a ^ tb;
      m0 = __ballot(stale(xa)) & go;
    } while (m0 != 0);
  }
  return xa;
}
__device__ __forceinline__ void gather4(f32x4 (&o)[4], v4u srd, int off, v4u tb) {
  v4u a, b, c, d, xa, xb, xc, xd;
  asm volatile("" : "=v"(a), "=v"(b), "=v"(c), "=v"(d));
  u64 m0 = ~0ull, m1 = ~0ull, m2 = ~0ull, m3 = ~0ull, go = ~0ull;
  unsigned left = SPIN_CAP;
#pragma nounroll
  do {
    asm volatile("s_mov_b64 exec, %7\n\t"
                 "buffer_load_dwordx4 %0, %6, %11, 0 offen sc1\n\t"
                 "s_mov_b64 exec, %8\n\t"
                 "buffer_load_dwordx4 %1, %6, %11, 0 offen offset:1024 sc1\n\t"
                 "s_mov_b64 exec, %9\n\t"
                 "buffer_load_dwordx4 %2, %6, %11, 0 offen offset:2048 sc1\n\t"
                 "s_mov_b64 exec, %10\n\t"
                 "buffer_load_dwordx4 %3, %6, %11, 0 offen offset:3072 sc1\n\t"
                 "s_mov_b64 exec, -1\n\t"
                 "s_sub_u32 %4, %4, 1\n\t"
                 "s_cselect_b64 %5, 0, %5\n\t"
                 "s_waitcnt vmcnt(0)"
                 : "+v"(a), "+v"(b), "+v"(c), "+v"(d), "+s"(left), "+s"(go)
                 : "v"(off), "s"(m0), "s"(m1), "s"(m2), "s"(m3), "s"(srd)
                 : "memory", "scc");
    xa = a ^ tb; xb = b ^ tb; xc = c ^ tb; xd = d ^ tb;
    if ((__ballot(stale((xa | xb) | (xc | xd))) & go) == 0) break;
    m0 = __ballot(stale(xa)) | 1; m1 = __ballot(stale(xb)) | 1; m2 = __ballot(stale(xc)) | 1; m3 = __ballot(stale(xd)) | 1;
  } while (true);
  o[0] = __builtin_bit_cast(f32x4, xa); o[1] = __builtin_bit_cast(f32x4, xb); o[2] = __builtin_bit_cast(f32x4, xc); o[3] = __builtin_bit_cast(f32x4, xd);
}

template <int DUE, int SL = 1> __device__ __forceinline__ void pace(unsigned t0) {
  asm volatile("1:\n\ts_memrealtime vcc\n\ts_waitcnt lgkmcnt(0)\n\ts_sub_u32 vcc_lo, vcc_lo, %0\n\ts_cmp_ge_i32 vcc_lo, %1\n\t"
               "s_cbranch_scc1 2f\n\ts_sleep %2\n\ts_branch 1b\n2:" :: "s"(t0), "i"(DUE), "i"(SL) : "vcc", "scc");
}
template <int DUE, bool WATCH> __device__ __forceinline__ void pace(unsigned t0, int sa, unsigned one) {
  unsigned tmp;
  if constexpr (WATCH)
    asm volatile("1:\n\ts_memrealtime vcc\n\tds_read_b32 %0, %3\n\ts_waitcnt lgkmcnt(0)\n\tv_readfirstlane_b32 vcc_hi, %0\n\ts_sub_u32 vcc_lo, vcc_lo, %1\n\ts_lshr_b32 vcc_hi, vcc_hi, %6\n\t"
                 "s_sub_u32 vcc_lo, vcc_lo, vcc_hi\n\ts_cmp_ge_i32 vcc_lo, %2\n\ts_cbranch_scc1 2f\n\ts_sleep 1\n\ts_branch 1b\n"
                 "2:\n\ts_getreg_b32 vcc_hi, hwreg(HW_REG_IB_STS, 0, 4)\n\ts_cmp_le_u32 vcc_hi, %5\n\ts_cbranch_scc1 3f\n\ts_mov_b64 exec, 1\n\tds_add_u32 %3, %4\n\ts_mov_b64 exec, -1\n3:"
                 : "=&v"(tmp) : "s"(t0), "i"(DUE), "v"(sa), "v"(one), "i"(ADP), "i"(ASH) : "vcc", "scc");
  else
    asm volatile("1:\n\ts_memrealtime vcc\n\tds_read_b32 %0, %3\n\ts_waitcnt lgkmcnt(0)\n\tv_readfirstlane_b32 vcc_hi, %0\n\ts_sub_u32 vcc_lo, vcc_lo, %1\n\ts_lshr_b32 vcc_hi, vcc_hi, %4\n\t"
                 "s_sub_u32 vcc_lo, vcc_lo, vcc_hi\n\ts_cmp_ge_i32 vcc_lo, %2\n\ts_cbranch_scc1 2f\n\ts_sleep 1\n\ts_branch 1b\n2:"
                 : "=&v"(tmp) : "s"(t0), "i"(DUE), "v"(sa), "i"(ASH) : "vcc", "scc");
}
template <int OFF> __device__ __forceinline__ void load_new(v4u& dst, int voff, v4u srd) {
  asm volatile("s_mov_b32 vcc_lo, %3\n\tbuffer_load_dwordx4 %0, %1, %2, vcc_lo offen nt" : "=v"(dst) : "v"(voff), "s"(srd), "i"(OFF) : "vcc");
}
template <int OFF> __device__ __forceinline__ void load_re(v4u& dst, int voff, v4u srd) {
  asm volatile("s_mov_b32 vcc_lo, %3\n\tbuffer_load_dwordx4 %0, %1, %2, vcc_lo offen nt" : "+v"(dst) : "v"(voff), "s"(srd), "i"(OFF) : "vcc");
}
template <int CNT, bool FIRST> __device__ __forceinline__ void qk_step(f32x4& d, v4u q, v4u k) {
  if constexpr (FIRST) asm volatile("s_waitcnt vmcnt(%3)\n\tv_mfma_f32_16x16x32_bf16 %0, %1, %2, 0" : "=&v"(d) : "v"(q), "v"(k), "i"(CNT));
  else asm volatile("s_waitcnt vmcnt(%3)\n\tv_mfma_f32_16x16x32_bf16 %0, %1, %2, %0" : "+v"(d) : "v"(q), "v"(k), "i"(CNT));
}
template <int CNT, bool FIRST> __device__ __forceinline__ void pv_step(f32x4& d0, f32x4& d1, v4u p0, v4u p1, v4u v) {
  if constexpr (FIRST) asm volatile("s_waitcnt vmcnt(%5)\n\ts_nop 0\n\tv_mfma_f32_16x16x32_bf16 %0, %2, %4, 0\n\tv_mfma_f32_16x16x32_bf16 %1, %3, %4, 0"
                                    : "=&v"(d0), "=&v"(d1) : "v"(p0), "v"(p1), "v"(v), "i"(CNT));
  else asm volatile("s_waitcnt vmcnt(%5)\n\ts_nop 0\n\tv_mfma_f32_16x16x32_bf16 %0, %2, %4, %0\n\tv_mfma_f32_16x16x32_bf16 %1, %3, %4, %1"
                    : "+v"(d0), "+v"(d1) : "v"(p0), "v"(p1), "v"(v), "i"(CNT));
}
__device__ __forceinline__ void undef(v4u& x) { asm volatile("" : "=v"(x)); }
template <int M> __device__ __forceinline__ void fold(float (&sc)[2], f32x4& d) {
  asm volatile("" : "+v"(d));
#pragma unroll
  for (int h = 0; h < 2; ++h) { sc[h] = put8<M>(sc[h], d[2 * h + (M & 1)] + dpp<XOR8>(d[2 * h + 1 - (M & 1)])); asm volatile("" : "+v"(sc[h])); }
}
constexpr int koff(int n) { return (n >> 2) * 524288 + (n & 3) * 128; }
constexpr int voffs(int n) { return (n >> 2) * 524288 + (n & 3) * 1024; }
constexpr int knext(int n) { return n < 24 ? n + WIN : (n & 1) ? 24 + WIN + (n - 24) / 2 : -1; }
constexpr int vissued(int n) { return (n & ~1) + WIN < 64 ? (n & ~1) + WIN : 64; }
constexpr int vnext(int n) { return ((n & 1) && vissued(n) < 64) ? vissued(n) : -1; }
constexpr int inflight(int n) { return (n >= 32 ? vissued(n) : n >= 24 ? 24 + WIN + (n - 24) / 2 : n + WIN) - n - 1; }
constexpr int slot(int n) { return n * TAU / 16; }

extern "C" __global__ void __launch_bounds__(1024, 4) k_fused(const unsigned short* kA, const unsigned short* vA, const unsigned short* kB, const unsigned short* vB,
                                                  const unsigned short* x, const unsigned short* wqA, const unsigned short* wqB, unsigned char* ws0,
                                                  const unsigned short* woA, const unsigned short* woB, const unsigned short* gw, unsigned short* out) {
  __shared__ __attribute__((aligned(16))) float lds[L_END];
  LAS float* L = (LAS float*)lds;
  LAS unsigned* ctl = (LAS unsigned*)(L + L_CTL);
  const int b = blockIdx.x, t = b & 7, j = b >> 3, tid = threadIdx.x;
  const int w = __builtin_amdgcn_readfirstlane(tid >> 6);
  const int tp = t ^ 1;
  if (__builtin_expect(w >= 4, 0)) {
    constexpr int W = WIN;
    const int sw = w - 4, u = sw >> 2, sub = sw & 3;
    const int gbase = (j * 4 + sub) * 4096;
    int kv = gbase + (tid & 7) * 512 + ((tid >> 3) & 1) * 64 + ((tid >> 4) & 3) * 16;
    const size_t hoff = (size_t)(u < 2 ? 2 * t + u : t) * SK * 256;
    asm volatile("" : "+v"(kv));
    const v4u kp = mksrd((u < 2 ? kA : kB) + hoff, 4 << 20), vp = mksrd((u < 2 ? vA : vB) + hoff, 4 << 20);
    v4u a[W];
    if (u == 0) {
      SFOR(n, 0, PRE) load_new<koff(n)>(a[n], kv, kp); SEND
    } else {
      SFOR(n, 0, PRE) undef(a[n]); SEND
    }
    WG_BARRIER();
    const unsigned tstart = __builtin_amdgcn_readfirstlane(ctl[C_T0]);
    const unsigned tw = tstart + ((u * W + SOFF - (u == 0 ? GAPS : 0)) * TAUW) / 16;
    if (u > 0) {
      pace<-60, 16>(tw);
      SFOR(n, 0, PRE)
        if constexpr (n % 2 == 0) pace<n * TAUW / 16>(tw);
        load_re<koff(n)>(a[n], kv, kp);
      SEND
    }
    SFOR(n, PRE, W)
      if constexpr (n % 2 == 0) pace<n * TAUW / 16>(tw);
      load_new<koff(n)>(a[n], kv, kp);
    SEND
    unsigned char* ws = ws_area(ws0);
    const int qh[2] = {u, u + (u >> 1)};
    LAS unsigned* pbuf = (LAS unsigned*)(L + L_P + sw * 128);
    lds_wait<2>(ctl + C_QR, 4);
    const unsigned tr = tstart + ((3 * W + SOFF) * TAUW + (u * (64 - W) - W) * TAU) / 16;
    int sa = (int)(size_t)(ctl + C_SL);
    unsigned one = 1;
    asm volatile("" : "+v"(sa), "+v"(one));
    float sc[2] = {0.f, 0.f}, mw[2];
    f32x4 acc[2];
    int vv;
    {
      const int lane = fresh_lane(), g = lane >> 4, c = lane & 15;
      vv = gbase + lane * 16;
      v4u q[4];
#pragma unroll
      for (int s = 0; s < 4; ++s) q[s] = ((const LAS v4u*)(L + L_Q))[(u + ((c >> 1) & (u >> 1))) * 32 + 8 * s + 4 * (c & 1) + g];
      pace<slot(W) - 60, 16>(tr);
      SFOR(n, 0, 32)
        constexpr int m = n >> 2, m2 = knext(n);
        qk_step<inflight(n), (n & 3) == 0>(acc[m & 1], q[n & 3], a[n % W]);
        if constexpr (m2 >= 0) {
          if constexpr (m2 % 2 == 0) pace<slot(m2), true>(tr, sa, one);
          if constexpr (m2 < 32) load_re<koff(m2)>(a[m2 % W], kv, kp); else load_re<voffs(m2 - 32)>(a[m2 % W], vv, vp);
        } else asm volatile("s_nop 1");
        if constexpr (n >= 6 && (n & 3) == 2) fold<m - 1>(sc, acc[(m - 1) & 1]);
      SEND
      asm volatile("s_nop 7");
      fold<7>(sc, acc[1]);
    }
    v4u tb;
    int ooff, ow0, ow1, ord, cadr;
    unsigned keep;
    {
      const int lane = fresh_lane(), g = lane >> 4, c = lane & 15;
      constexpr float C2 = 0.0625f * 1.4426950408889634f;
#pragma unroll
      for (int h = 0; h < 2; ++h) {
        const float s = sc[h] * C2;
        const float m = wave_max(s);
        const float p = __builtin_amdgcn_exp2f(s - m);
        const unsigned pb = pk2bf(p, p);
        pbuf[h * 64 + (lane & 1) * 32 + (lane >> 1)] = pb;
        const float l = wave_sum(bflo(pb));
        mw[h] = m;
        if (lane == 0) { L[L_ML + qh[h] * 8 + sub] = m; L[L_ML + qh[h] * 8 + 4 + sub] = l; }
        if (h == 0) {
          CBAR();
          pace<slot(28 + W), true>(tr, sa, one);
          load_re<voffs(28 + W - 32)>(a[28 % W], vv, vp); load_re<voffs(29 + W - 32)>(a[29 % W], vv, vp);
          asm volatile("" : "+v"(sc[1]));
        }
      }
      lds_signal(ctl + C_KD + u, lane);
      v4u mk;
#pragma unroll
      for (int e = 0; e < 4; ++e) mk[e] = ((g & 1) == (c >> 3) && ((c & 7) >> 1) == e) ? ((c & 1) ? 0xFFFF0000u : 0x0000FFFFu) : 0u;
      v2u pv[2][2];
#pragma unroll
      for (int h = 0; h < 2; ++h) pv[0][h] = *(const LAS v2u*)(pbuf + h * 64 + (lane & 32));
      asm volatile("" : "+v"(mk));
      pace<slot(30 + W), true>(tr, sa, one);
      load_re<voffs(30 + W - 32)>(a[30 % W], vv, vp); load_re<voffs(31 + W - 32)>(a[31 % W], vv, vp);
      SFOR(i, 0, 32)
        constexpr int n = 32 + i, m2 = vnext(n);
        if constexpr (i == 2) {
          tb = tagfields(load_tag(ws));
          if (u > 0) lds_wait<1>(ctl + C_GP, 1);
          ooff = WS_O + blk((t & 3) * 32 + (lane >> 3), ((t >> 2) ^ ctl[C_GP]) & 1) + ((j + 4 * (lane >> 3)) & 31) * 128 + (lane & 7) * 16;
          ow0 = (L_O + (qh[0] * 4 + sub) * 256 + 128 * (g >> 1) + 8 * c + 4 * (g & 1)) * 4; ow1 = ow0 + (u >> 1) * 4096;
          ord = L_O * 4 + lane * 16; cadr = (int)(size_t)(ctl + C_OD + u); keep = ~TM;
          asm volatile("" : "+v"(ooff), "+v"(ow0), "+v"(ow1), "+v"(ord), "+v"(cadr), "+v"(keep), "+s"(tb));
        }
        if constexpr (i == 4) {
          lds_wait<1>(ctl + C_KD + u, 4);
#pragma unroll
          for (int h = 0; h < 2; ++h) {
            const f32x4 m4 = *(const LAS f32x4*)(L + L_ML + qh[h] * 8);
            mw[h] = __builtin_amdgcn_exp2f(mw[h] - fmaxf(fmaxf(m4[0], m4[1]), fmaxf(m4[2], m4[3])));
            asm volatile("" : "+v"(mw[h]));
          }
        }
        if constexpr (i > 0 && vnext(n - 1) == 62) {
          lds_signal(ctl + C_VL, lane);
          __builtin_amdgcn_s_setprio(3);
        }
        if constexpr ((i & 1) == 0 && i + 2 < 32) {
#pragma unroll
          for (int h = 0; h < 2; ++h) pv[((i >> 1) + 1) & 1][h] = *(const LAS v2u*)(pbuf + h * 64 + (lane & 32) + i + 2);
        }
        const unsigned p0 = pv[(i >> 1) & 1][0][i & 1], p1 = pv[(i >> 1) & 1][1][i & 1];
        const v4u a0 = {p0 & mk[0], p0 & mk[1], p0 & mk[2], p0 & mk[3]}, a1 = {p1 & mk[0], p1 & mk[1], p1 & mk[2], p1 & mk[3]};
        pv_step<inflight(n), i == 0>(acc[0], acc[1], a0, a1, a[n % W]);
        if constexpr (m2 >= 0) {
          pace<slot(m2), true>(tr, sa, one);
          load_re<voffs(m2 - 32)>(a[m2 % W], vv, vp); load_re<voffs(m2 - 31)>(a[(m2 + 1) % W], vv, vp);
        }
      SEND
      asm volatile("s_nop 7" : "+v"(acc[0]), "+v"(acc[1]));
    }
    const rsrc_t rws = mkrsrc(ws, WS_END);
    LAS unsigned char* L8 = (LAS unsigned char*)L;
    *(LAS f32x4*)(L8 + ow0) = acc[0] * mw[0];
    *(LAS f32x4*)(L8 + ow1) = acc[1] * mw[1];
    unsigned arrv;
    asm volatile("s_mov_b64 exec, 1\n\tds_add_rtn_u32 %0, %1, %2\n\ts_mov_b64 exec, -1" : "=&v"(arrv) : "v"(cadr), "v"(one) : "memory");
    const LAS f32x4* O = (const LAS f32x4*)(L8 + ord);
    f32x4 r8[8];
#pragma unroll
    for (int k = 0; k < 8; ++k) r8[k] = O[512 + 64 * k];
    asm volatile("" : "+v"(r8[0]), "+v"(r8[1]), "+v"(r8[2]), "+v"(r8[3]), "+v"(r8[4]), "+v"(r8[5]), "+v"(r8[6]), "+v"(r8[7]), "+v"(arrv));
    const unsigned arr = __builtin_amdgcn_readfirstlane(arrv);
    if (__builtin_expect(arr + 4 * u == 11, 1)) {
      __builtin_amdgcn_raw_buffer_store_b128(seal((r8[0] + r8[1]) + (r8[2] + r8[3]), tb, keep), rws, ooff, 16 * 8192, AUX_SC1);
      __builtin_amdgcn_raw_buffer_store_b128(seal((r8[4] + r8[5]) + (r8[6] + r8[7]), tb, keep), rws, ooff, 24 * 8192, AUX_SC1);
    } else if (u == 1) {
      __builtin_amdgcn_s_setprio(0);
      if (arr == 3) {
        lds_wait<1>(ctl + C_OD, 4);
        v4u o0 = seal((O[0] + O[64]) + (O[128] + O[192]), tb), o1 = seal((O[256] + O[320]) + (O[384] + O[448]), tb);
        asm volatile("" : "+v"(o0), "+v"(o1));
        lds_wait<8>(ctl + C_KD + 2, 4);
        __builtin_amdgcn_s_sleep(40);
        lds_wait<2>(ctl + C_VL, 12);
        __builtin_amdgcn_s_sleep(24);
        __builtin_amdgcn_raw_buffer_store_b128(o0, rws, ooff, 0, AUX_SC1);
        __builtin_amdgcn_raw_buffer_store_b128(o1, rws, ooff, 8 * 8192, AUX_SC1);
        asm volatile(".Lkf_end4:");
      }
    } else if (u == 0) {
      __builtin_amdgcn_s_setprio(0);
      const int lane = fresh_lane();
      const rsrc_t rwo = mkrsrc(j < 16 ? woA : woB, HD * D * 2);
      const int woff = (512 * tp + 32 * (j & 15) + 8 * sub) * D * 2 + (lane < 56 ? lane : 55) * 16;
      const unsigned tb2 = tstart + ((3 * W + SOFF) * TAUW + (128 - 3 * W) * TAU) / 16;
      pace<slot(58) - 200, 64>(tb2);
      lds_wait<8>(ctl + C_GP, 1);
      const int gp = ctl[C_GP] & 1;
      pace<slot(62) - 25, 8>(tb2);
      pace<slot(62), false>(tb2, sa, one);
      lds_wait<1>(ctl + C_VL, 12);
      v4u wo[8];
      SFOR(k, 0, 4)
        pace<((62 + WD) * 16 + k * WSTEP) * TAU / 256, false>(tb2, sa, one);
        wo[2 * k] = __builtin_amdgcn_raw_buffer_load_b128(rwo, woff, 2 * k * D * 2, AUX_NT);
        wo[2 * k + 1] = __builtin_amdgcn_raw_buffer_load_b128(rwo, woff, (2 * k + 1) * D * 2, AUX_NT);
        SB();
      SEND
      const v4u srd = mksrd(ws, WS_END);
      const int poff = WS_O + blk((tp & 3) * 32 + j, (tp >> 2) ^ gp) + (((lane >> 1) + 4 * (j & 7)) & 31) * 128 + sub * 32 + (lane & 1) * 16;
      lds_wait<1>(ctl + C_OD + 2, 4);
      __builtin_amdgcn_s_sleep(10);
      const v4u o1 = ldsc(rws, poff);
      SB();
      lds_wait<1>(ctl + C_WN, 1);
      float wn = L[L_WN + (lane >> 1)];
      const int ps = (j >> 4) * 128 + tp * 16 + (j & 15);
      const int rb = 28 * sub + lane;
      int yoff = WS_Y + blk((rb >> 3) * 4 + (rb & 3), ((rb >> 2) & 1) ^ gp) + ps * 16;
      asm volatile("" : "+v"(wn), "+v"(yoff));
      unsigned wp[4][8];
#pragma unroll
      for (int r = 0; r < 4; ++r) {
#pragma unroll
        for (int k = 0; k < 4; ++k) {
          wp[r][2 * k] = __builtin_amdgcn_perm(wo[2 * r + 1][k], wo[2 * r][k], 0x05040100u);
          wp[r][2 * k + 1] = __builtin_amdgcn_perm(wo[2 * r + 1][k], wo[2 * r][k], 0x07060302u);
        }
        asm volatile("" : "+v"(wp[r][0]), "+v"(wp[r][1]), "+v"(wp[r][2]), "+v"(wp[r][3]), "+v"(wp[r][4]), "+v"(wp[r][5]), "+v"(wp[r][6]), "+v"(wp[r][7]));
      }
      float y[8];
#pragma unroll
      for (int c2 = 0; c2 < 8; ++c2) y[c2] = 0.f;
      int yw = (L_Y + sub * 448 + 8 * (lane < 56 ? lane : 55)) * 4, yr = (L_Y + 4 * (28 * sub + (lane < 28 ? lane : 27))) * 4;
      asm volatile("" : "+v"(y[0]), "+v"(y[1]), "+v"(y[2]), "+v"(y[3]), "+v"(y[4]), "+v"(y[5]), "+v"(y[6]), "+v"(y[7]), "+v"(yw), "+v"(yr));
      const f32x4 s = wn * __builtin_bit_cast(f32x4, gather1(o1, srd, poff, tb));
      float t0 = swap32_add(s[0], s[2]), t1 = swap32_add(s[1], s[3]);
      t0 = swap16_add(t0, t1);
      t0 += dpp<XOR8>(t0); t0 += dpp<ROR4>(t0); t0 += dpp<XOR2>(t0);
      const auto sw2 = __builtin_amdgcn_permlane16_swap(__float_as_uint(t0), __float_as_uint(t0), false, false);
      const unsigned pk = pk2bf(__uint_as_float(sw2[0]), __uint_as_float(sw2[1]));
#pragma unroll
      for (int r = 0; r < 4; ++r) {
        const unsigned op = __builtin_amdgcn_readlane(pk, (r >> 1) + 32 * (r & 1));
#pragma unroll
        for (int c2 = 0; c2 < 8; ++c2) y[c2] = dot2(wp[r][c2], op, y[c2]);
      }
      *(LAS f32x4*)(L8 + yw) = f32x4{y[0], y[1], y[2], y[3]};
      *(LAS f32x4*)(L8 + yw + 16) = f32x4{y[4], y[5], y[6], y[7]};
      WG_BARRIER_LDS();
      const LAS f32x4* Y = (const LAS f32x4*)(L8 + yr);
      const v4u yy = seal((Y[0] + Y[112]) + (Y[224] + Y[336]), tb, keep);
      if (lane < 28) __builtin_amdgcn_raw_buffer_store_b128(yy, rws, yoff, 0, AUX_SC1);
      asm volatile(".Lkf_end3:");
    }
    asm volatile(".Lkf_end1:");
    return;
  }
  __builtin_amdgcn_s_setprio(3);
  const int lane = tid & 63;
  u64 tag;
  v4u tb;
  unsigned char* ws;
  rsrc_t rws;
  v4u srd;
  {
    unsigned pfc = 0;
    {
      u64 pc; unsigned back, sz;
      asm volatile("s_getpc_b64 %0\n.Lkf_here:\n\ts_mov_b32 %1, .Lkf_here-k_fused\n\ts_mov_b32 %2, .Lkf_end1-k_fused\n\ts_max_u32 %2, %2, .Lkf_end2-k_fused\n\t"
                   "s_max_u32 %2, %2, .Lkf_end3-k_fused\n\ts_max_u32 %2, %2, .Lkf_end4-k_fused" : "=s"(pc), "=s"(back), "=s"(sz) :: "scc");
      if (lane < 3) pfc = __builtin_amdgcn_raw_buffer_load_b32(mkrsrc((const void*)(pc - back), sz), (j * 4 + w) * 192 + lane * 64, 0, 0);
    }
    const int part = j & 1, cg = (j & 15) >> 1;
    const int r0 = part * 224 + w * 56 + (lane >> 3);
    int wqo = (r0 * HD + 512 * tp + cg * 64 + 8 * (lane & 7)) * 2;
    asm volatile("" : "+v"(wqo));
    SB();
    u64 tk;
    asm volatile("s_memrealtime %0" : "=s"(tk) : "s"(wqA));
    const rsrc_t rwq = mkrsrc(j < 16 ? wqA : wqB, D * HD * 2);
    const rsrc_t rx = mkrsrc(x, D * 2);
    v4u wv[7];
    unsigned xi[7];
#pragma unroll
    for (int i = 0; i < 7; ++i) {
      wv[i] = __builtin_amdgcn_raw_buffer_load_b128(rwq, wqo, i * 8 * HD * 2, AUX_NT);
      if (i < 6) { SB(); __builtin_amdgcn_s_sleep(4); SB(); }
    }
#pragma unroll
    for (int i = 0; i < 7; ++i) xi[i] = __builtin_amdgcn_raw_buffer_load_b32(rx, (r0 >> 1) * 4 + i * 16, 0, 0);
    asm volatile("s_waitcnt lgkmcnt(0)" : "+s"(tk));
    if (tid < C_N) ctl[tid] = tid == C_T0 ? (unsigned)tk : 0u;
    SB();
    WG_BARRIER_LDS();
    SB();
    ws = ws_area(ws0); rws = mkrsrc(ws, WS_END); srd = mksrd(ws, WS_END);
    tag = load_tag(ws);
    asm volatile("" : "+s"(tag));
    SB();
    tb = tagfields(tag);
#pragma unroll
    for (int i = 0; i < 7; ++i) asm volatile("" : "+v"(wv[i]), "+v"(xi[i]));
    f32x2 acc[4];
#pragma unroll
    for (int k = 0; k < 4; ++k) acc[k] = f32x2{0.f, 0.f};
#pragma unroll
    for (int i = 0; i < 7; ++i) {
      const float xv = (lane & 8) ? bfhi(xi[i]) : bflo(xi[i]);
#pragma unroll
      for (int k = 0; k < 4; ++k) acc[k] = __builtin_elementwise_fma(f32x2{xv, xv}, f32x2{bflo(wv[i][k]), bfhi(wv[i][k])}, acc[k]);
    }
    const float c0 = swap32_add(acc[0][0], acc[2][0]), c1 = swap32_add(acc[0][1], acc[2][1]), c2 = swap32_add(acc[1][0], acc[3][0]), c3 = swap32_add(acc[1][1], acc[3][1]);
    float e0 = swap16_add(c0, c2), e1 = swap16_add(c1, c3);
    e0 += dpp<XOR8>(e0); e1 += dpp<XOR8>(e1);
    if ((lane & 8) == 0) *(LAS f32x2*)(L + L_RED + w * 64 + 8 * (lane & 7) + 4 * (lane >> 5) + 2 * ((lane >> 4) & 1)) = f32x2{e0, e1};
    if (lds_arrive(ctl + C_QP, lane) == 3) {
      const LAS f32x4* R = (const LAS f32x4*)(L + L_RED) + (lane & 15);
      const f32x4 s = (R[0] + R[16]) + (R[32] + R[48]);
      if (lane < 16) __builtin_amdgcn_raw_buffer_store_b128(seal(s, tb), rws, WS_QG + tp * 8192 + part * 4096 + (j >> 4) * 2048 + cg * 256 + lane * 16, 0, AUX_SC1);
    }
    pace<450, 4>((unsigned)tk);
    asm volatile("" :: "v"(pfc));
    v4u g0 = {0u, 0u, 0u, 0u}, g1 = {0u, 0u, 0u, 0u};
    bool ok = false;
#pragma nounroll
    for (unsigned spins = 0; spins < SPIN_CAP; ++spins) {
      CBAR();
      if (!ok) {
        g0 = ldsc(rws, WS_QG + t * 8192 + tid * 16) ^ tb;
        g1 = ldsc(rws, WS_QG + t * 8192 + 4096 + tid * 16) ^ tb;
      }
      ok = !stale(g0 | g1);
      if (__all(ok)) break;
      __builtin_amdgcn_s_sleep(8);
    }
    const f32x4 s = __builtin_bit_cast(f32x4, g0) + __builtin_bit_cast(f32x4, g1);
    *(LAS v2u*)(L + L_Q + tid * 2) = v2u{pk2bf(s[0], s[1]), pk2bf(s[2], s[3])};
    lds_signal(ctl + C_QR, lane);
  }
  __builtin_amdgcn_s_setprio(1);
  const bool summer = b < NRED && w == 0;
  float gate = 0.f;
  if (summer) {
    float gs = 0.f;
#pragma unroll
    for (int i = 0; i < 7; ++i) gs += rbf(bf2f(x[lane + 64 * i]) * bf2f(gw[lane + 64 * i]));
    gs = rbf(wave_sum(gs));
    gate = rbf(1.0f / (1.0f + __expf(-gs)));
    if (lane >= 32) gate = rbf(1.0f - gate);
    asm volatile("" : "+v"(gate));
  }
  if (b == 0 && w == 1) {
    if (lane < NCAL) __builtin_amdgcn_raw_buffer_store_b32(0u, rws, WS_CAL + (lane & 7) * 4096 + (lane >> 3) * 640, 0, AUX_SC1);
    asm volatile("s_waitcnt vmcnt(0)" ::: "memory");
    unsigned best0 = ~0u, best1 = ~0u;
#pragma nounroll
    for (int i = 0; i < NCAL; ++i) {
      const u64 p = (u64)ws + WS_CAL + (i & 7) * 4096 + (i >> 3) * 640;
      u64 c0, c1; unsigned v;
      asm volatile("s_memtime %0\n\ts_waitcnt lgkmcnt(0)\n\ts_load_dword %2, %3, 0x0\n\ts_waitcnt lgkmcnt(0)\n\ts_memtime %1\n\ts_waitcnt lgkmcnt(0)" : "=&s"(c0), "=&s"(c1), "=&s"(v) : "s"(p) : "memory");
      const unsigned dt = (unsigned)(c1 - c0);
      if (i & 2) best1 = min(best1, dt); else best0 = min(best0, dt);
    }
    if (lane == 0) __builtin_amdgcn_raw_buffer_store_b128(v4u{((best1 < best0 ? 1u : 0u) << 10) | tb[0], tb[1], tb[2], tb[3]}, rws, WS_GP, 0, AUX_SC1);
  }
  if (w == 2) {
    lds_wait<64>(ctl + C_VL, 4);
    const v4u gv = gather1(ldsc(rws, WS_GP), srd, WS_GP, tb);
    if (lane == 0) ctl[C_GP] = 2u | ((gv[0] >> 10) & 1u);
  }
  {
    lds_wait<64>(ctl + C_VL, 8);
    lds_wait<8>(ctl + C_KD + 2, 4);
    lds_wait<1>(ctl + C_KD + (w >> 1 ? 2 : w), 4);
    const f32x4 m4 = *(const LAS f32x4*)(L + L_ML + w * 8), l4 = *(const LAS f32x4*)(L + L_ML + w * 8 + 4);
    const float M = fmaxf(fmaxf(m4[0], m4[1]), fmaxf(m4[2], m4[3]));
    float Ls = 0.f;
#pragma unroll
    for (int s = 0; s < 4; ++s) Ls = fmaf(__builtin_amdgcn_exp2f(m4[s] - M), l4[s], Ls);
    const f32x4 ml = {M, __uint_as_float(__float_as_uint(M) << 10), Ls, __uint_as_float(__float_as_uint(Ls) << 10)};
    if (lane == 0) __builtin_amdgcn_raw_buffer_store_b128(seal(ml, tb), rws, WS_ML + (t * 4 + w) * 512 + j * 16, 0, AUX_SC1);
  }
  if (w == 3) {
    const int mloff = WS_ML + (tp * 4 + (j >> 3)) * 512 + (lane & 31) * 16;
    __builtin_amdgcn_s_sleep(40);
    const v4u mlv = gather1(ldsc(rws, mloff), srd, mloff, tb);
    const float m = __uint_as_float(mlv[0] | ((mlv[1] >> 10) & TM)), l = __uint_as_float(mlv[2] | ((mlv[3] >> 10) & TM));
    const float wt = __builtin_amdgcn_exp2f(m - wave_max(m));
    const float wn = wt * (2.0f / wave_sum(wt * l));
    if (lane < 32) L[L_WN + lane] = wn;
    lds_signal(ctl + C_WN, lane);
  }
  if (summer) {
    GAS unsigned short* op = (GAS unsigned short*)out + b * 4 + ((lane >> 3) & 1) * 2 + ((lane >> 4) & 1);
    lds_wait<1>(ctl + C_GP, 1);
    int yo = WS_Y + blk((b >> 3) * 4 + (b & 3), ((b >> 2) ^ ctl[C_GP]) & 1) + lane * 16;
    asm volatile("" : "+v"(op), "+v"(yo));
    WG_BARRIER();
    __builtin_amdgcn_s_sleep(20);
    f32x4 y[4];
    gather4(y, srd, yo, tb);
    const f32x4 sa = y[0] + y[1], sb = y[2] + y[3];
    const float v0 = swap32_add(sa[0], sb[0]), v1 = swap32_add(sa[1], sb[1]), v2 = swap32_add(sa[2], sb[2]), v3 = swap32_add(sa[3], sb[3]);
    const float z0 = swap16_add(v0, v1), z1 = swap16_add(v2, v3);
    float z = ((lane & 8) ? z1 : z0) + dpp<XOR8>((lane & 8) ? z0 : z1);
    z += dpp<XOR7>(z); z += dpp<XOR2>(z); z += dpp<XOR1>(z);
    z = rbf(rbf(z) * gate);
    z = swap32_add(z, z);
    if ((lane & 39) == 0) *op = f2bf(z);
    if (b == 0 && lane == 0) __builtin_amdgcn_raw_buffer_store_b64(v2u{(unsigned)tag, (unsigned)(tag >> 32)}, rws, WS_EPOCH, 0, AUX_SC1);
  }
  asm volatile(".Lkf_end2:");
}

extern "C" void kernel_launch(void* const* d_in, const int* in_sizes, int n_in, void* d_out, int out_size, void* d_ws, size_t ws_size, hipStream_t stream) {
  typedef const unsigned short* cp;
  k_fused<<<256, 1024, 0, stream>>>((cp)d_in[1], (cp)d_in[2], (cp)d_in[6], (cp)d_in[7], (cp)d_in[0], (cp)d_in[3], (cp)d_in[8], (unsigned char*)d_ws,
                                    (cp)d_in[4], (cp)d_in[9], (cp)d_in[10], (unsigned short*)d_out);
}
```
